```python
import torch

def _softmax(x, axis=-1):
    return torch.softmax(x, dim=axis)
def _gelu(x):
    return torch.nn.functional.gelu(x, approximate="tanh")
def _silu(x):
    return torch.nn.functional.silu(x)
def _sigmoid(x):
    return torch.sigmoid(x)
def _layernorm(x, axis=-1, eps=1e-5):
    m = x.mean(dim=axis, keepdim=True)
    v = x.var(dim=axis, keepdim=True, unbiased=False)
    return (x - m) / torch.sqrt(v + eps)
def _scatter_add(tgt, idx, src, axis=0):
    return tgt.clone().index_add_(axis, idx, src)
def _segment_sum(src, seg, n):
    out = torch.zeros((n,) + src.shape[1:], dtype=src.dtype)
    return out.index_add_(0, seg, src)
def _pad(x, pw):
    flat = tuple(v for pair in reversed(pw) for v in pair)
    return torch.nn.functional.pad(x, flat)

kernel_name = 'standalone__batchnorm__sampling'

def setup_inputs():

    _w = (lambda a: (lambda t: t.to(torch.bfloat16) if t.is_floating_point() else t)(torch.as_tensor(a)))
    B, S, C = 1024, 4, 576
    K = 3
    return {
        "x": _w((lambda *s: torch.randn(*s, dtype=torch.bfloat16) * 0.05)(B, S, C)),
        "w": _w((lambda *s: torch.randn(*s, dtype=torch.bfloat16) * 0.05)(K, C, C) * (1.0 / (K * C) ** 0.5)),
        "mean": _w((lambda *s: torch.randn(*s, dtype=torch.bfloat16) * 0.05)(C) * 0.1),
        "var": _w(torch.abs((lambda *s: torch.randn(*s, dtype=torch.bfloat16) * 0.05)(C)) + 0.5),
        "g": _w((lambda *s: torch.randn(*s, dtype=torch.bfloat16) * 0.05)(C) + 1.0),
        "b": _w((lambda *s: torch.randn(*s, dtype=torch.bfloat16) * 0.05)(C) * 0.1),
    }

def reference(x, w, mean, var, g, b):
    K = 3
    P = K // 2
    S = x.shape[1]
    xp = _pad(x, ((0, 0), (P, P), (0, 0)))
    y = sum(torch.einsum('bsc,cd->bsd', xp[:, k:k + S, :], w[k]) for k in range(K))
    inv_std = 1.0 / torch.sqrt(var + 1e-5)
    y = (y - mean) * (g * inv_std) + b
    return _silu(y)
```

```cpp
#include <hip/hip_runtime.h>
#include <hip/hip_bf16.h>

typedef __attribute__((ext_vector_type(4))) __bf16 bf16x4;
typedef __attribute__((ext_vector_type(8))) __bf16 bf16x8;
typedef __attribute__((ext_vector_type(2))) __bf16 bf16x2;
typedef __attribute__((ext_vector_type(4))) float f32x4;
typedef __attribute__((ext_vector_type(2))) float f32x2;
typedef __attribute__((ext_vector_type(4))) unsigned u32x4;
typedef __attribute__((ext_vector_type(2))) unsigned u32x2;
typedef __attribute__((address_space(3))) char* lds_ptr;
typedef __attribute__((address_space(3))) unsigned* lds_u32p;
typedef __attribute__((address_space(3))) bf16x4* lds_b4p;
typedef __attribute__((address_space(3))) bf16x8* lds_b8p;

constexpr int NB = 1024, C = 576;
constexpr int ROWB = C * 2;
constexpr int BATB = 4 * ROWB;
constexpr int BT = 40;
constexpr int XSLOT = 160 * 128, NXS = 4;
constexpr int WSLOT = 96 * 128, NWS = 6;
constexpr int WBASE = XSLOT * NXS;
constexpr int LDS_BYTES = WBASE + WSLOT * NWS;
constexpr int NSTEP = C / 32;

#define BAR() asm volatile("s_barrier" ::: "memory")
#define WAITV(n) asm volatile("s_waitcnt vmcnt(%0)" ::"i"(n) : "memory")

struct Frags { bf16x8 x[5]; bf16x8 w[3][2]; };

__device__ __forceinline__ void read_frags(Frags& f, lds_ptr xa, lds_ptr w0, lds_ptr w1) {
#pragma unroll
  for (int t = 0; t < 3; ++t) {
    bf16x4 a = __builtin_amdgcn_ds_read_tr16_b64_v4bf16((lds_b4p)(w0 + t * 4096));
    bf16x4 b = __builtin_amdgcn_ds_read_tr16_b64_v4bf16((lds_b4p)(w0 + t * 4096 + 512));
    f.w[t][0] = __builtin_shufflevector(a, b, 0, 1, 2, 3, 4, 5, 6, 7);
    bf16x4 c = __builtin_amdgcn_ds_read_tr16_b64_v4bf16((lds_b4p)(w1 + t * 4096));
    bf16x4 d = __builtin_amdgcn_ds_read_tr16_b64_v4bf16((lds_b4p)(w1 + t * 4096 + 512));
    f.w[t][1] = __builtin_shufflevector(c, d, 0, 1, 2, 3, 4, 5, 6, 7);
  }
#pragma unroll
  for (int s = 0; s < 5; ++s) f.x[s] = *(lds_b8p)(xa + s * 2048);
}

#define MF(acc, a, b) acc = __builtin_amdgcn_mfma_f32_16x16x32_bf16(a, b, acc, 0, 0, 0)

__device__ __forceinline__ void mma(f32x4 (&y)[4][2], f32x4 (&p)[3][2], const Frags& f) {
#pragma unroll
  for (int u = 0; u < 2; ++u) {
    MF(y[0][u], f.w[1][u], f.x[0]);
    MF(y[1][u], f.w[0][u], f.x[0]);
    MF(y[2][u], f.w[0][u], f.x[1]);
    MF(y[3][u], f.w[0][u], f.x[2]);
    MF(p[0][u], f.w[0][u], f.x[4]);
    MF(y[0][u], f.w[2][u], f.x[1]);
    MF(y[1][u], f.w[1][u], f.x[1]);
    MF(y[2][u], f.w[1][u], f.x[2]);
    MF(y[3][u], f.w[1][u], f.x[3]);
    MF(p[1][u], f.w[1][u], f.x[4]);
    MF(y[1][u], f.w[2][u], f.x[2]);
    MF(y[2][u], f.w[2][u], f.x[3]);
    MF(p[2][u], f.w[2][u], f.x[4]);
  }
}

__device__ __forceinline__ void interleave() {
#pragma unroll
  for (int i = 0; i < 17; ++i) {
    __builtin_amdgcn_sched_group_barrier(0x008, 1, 0);
    __builtin_amdgcn_sched_group_barrier(0x100, 1, 0);
  }
  __builtin_amdgcn_sched_group_barrier(0x008, 9, 0);
}

__device__ __forceinline__ float bf2f(unsigned short v) { return __uint_as_float((unsigned)v << 16); }

__device__ __forceinline__ unsigned pack2(float a, float b) {
  bf16x2 v = {(__bf16)a, (__bf16)b};
  return __builtin_bit_cast(unsigned, v);
}

__device__ __forceinline__ float silu(float z) {
  return z * __builtin_amdgcn_rcpf(1.0f + __builtin_amdgcn_exp2f(z * -1.44269504088896340736f));
}

__global__ __launch_bounds__(512, 2) void conv_bn_silu(const char* __restrict__ x, const char* __restrict__ w,
                                                        const unsigned short* __restrict__ mean,
                                                        const unsigned short* __restrict__ var,
                                                        const unsigned short* __restrict__ gam,
                                                        const unsigned short* __restrict__ bet,
                                                        char* __restrict__ out) {
  __shared__ __attribute__((aligned(1024))) char smem[LDS_BYTES];
  lds_ptr lds = (lds_ptr)smem;

  const int bid = blockIdx.x;
  const int xcd = bid & 7, slot = bid >> 3;
  const int q = xcd >> 1, h = xcd & 1;
  const int nr = q < 2 ? 7 : 6;
  const int rstart = q * 7 - (q == 3 ? 1 : 0);
  const int half = q < 2 ? 32 : 27;
  const int idx = h * half + slot;
  if (slot >= half || idx >= nr * 9) return;
  const int cb = q < 2 ? idx / 7 : idx / 6;
  const int rb = rstart + idx - cb * nr;
  const int bfirst = rb * BT;
  const int b0 = bfirst < NB - BT ? bfirst : NB - BT;

  const int wid = __builtin_amdgcn_readfirstlane(threadIdx.x >> 6);
  const int lane = threadIdx.x & 63;

  if (wid >= 4) {
    const int lw = wid - 4, par = lw & 1, hi = lw >> 1;
    const __amdgpu_buffer_rsrc_t rx = __builtin_amdgcn_make_buffer_rsrc((void*)x, 0, NB * BATB, 0x00020000);
    const __amdgpu_buffer_rsrc_t rw = __builtin_amdgcn_make_buffer_rsrc((void*)w, 0, 3 * C * ROWB, 0x00020000);
    const int rl = lane >> 3;
    const int xsw = ((lane & 7) ^ (4 * par | (lane >> 4))) * 16;
    const int vxm = (par * 8 + rl) * BATB + xsw;
    const int vxf = rl * ROWB + xsw;
    const int vw = rl * ROWB + ((lane & 7) ^ ((((lane >> 4) & 1) | (par << 1)) << 1)) * 16;
    const int sxm = b0 * BATB + hi * ROWB;
    const int sxf = (b0 + 32) * BATB + 8 * lw * ROWB;
    const int sw = 8 * lw * ROWB + cb * 128;
    const int dxm = hi * 2048 + par * 1024;
    const int dxf = (hi * 5 + 4) * 2048 + par * 1024;
    const int dw = WBASE + lw * 1024;

    auto ldx = [&](int c, int i) {
      if (i < 4)
        __builtin_amdgcn_raw_ptr_buffer_load_lds(rx, (lds_u32p)(lds + (c % NXS) * XSLOT + dxm + (2 * i + (i >> 1)) * 2048), 16, vxm,
                                                 sxm + (i >> 1) * 16 * BATB + (i & 1) * 2 * ROWB + c * 128, 0, 0);
      else
        __builtin_amdgcn_raw_ptr_buffer_load_lds(rx, (lds_u32p)(lds + (c % NXS) * XSLOT + dxf), 16, vxf, sxf + c * 128, 0, 0);
    };
    auto ldw = [&](int j) {
#pragma unroll
      for (int t = 0; t < 3; ++t)
        __builtin_amdgcn_raw_ptr_buffer_load_lds(rw, (lds_u32p)(lds + (j % NWS) * WSLOT + dw + t * 4096), 16, vw,
                                                 sw + (t * C + 32 * j) * ROWB, 0, 0);
    };
    auto interval = [&](int i) {
      if (i + 6 >= 0 && i + 6 < NSTEP) ldw(i + 6);
      if (i & 1) {
        const int c = (i + 7) / 2;
        if (c < 9) { ldx(c, 0); ldx(c, 1); ldx(c, 2); }
      } else {
        const int c = (i + 6) / 2;
        if (c < 9) { ldx(c, 3); ldx(c, 4); }
      }
    };
#pragma unroll
    for (int i = -7; i < 0; ++i) interval(i);
    WAITV(28);
    BAR();
#pragma unroll
    for (int j = 0; j < NSTEP - 1; ++j) {
      if (j <= 11) WAITV(22);
      else if (j == 12) WAITV(19);
      else if (j == 13) WAITV(14);
      else if (j == 14) WAITV(8);
      else if (j == 15) WAITV(3);
      else WAITV(0);
      BAR();
      interval(j);
    }
    return;
  }

  const int wb = wid & 1, wc = wid >> 1;
  const int i16 = lane & 15, g = lane >> 4;

  const int dpar = cb * 64 + wc * 32 + 4 * g;
  float sc[2][4], sh[2][4];
#pragma unroll
  for (int u = 0; u < 2; ++u) {
    const u32x2 m2 = *(const u32x2*)(mean + dpar + 16 * u);
    const u32x2 v2 = *(const u32x2*)(var + dpar + 16 * u);
    const u32x2 g2 = *(const u32x2*)(gam + dpar + 16 * u);
    const u32x2 b2 = *(const u32x2*)(bet + dpar + 16 * u);
#pragma unroll
    for (int r = 0; r < 4; ++r) {
      const int sft = (r & 1) * 16;
      const float mm = bf2f((unsigned short)(m2[r >> 1] >> sft));
      const float vv = bf2f((unsigned short)(v2[r >> 1] >> sft));
      const float gg = bf2f((unsigned short)(g2[r >> 1] >> sft));
      const float bb = bf2f((unsigned short)(b2[r >> 1] >> sft));
      sc[u][r] = gg * __builtin_amdgcn_rsqf(vv + 1e-5f);
      sh[u][r] = bb - mm * sc[u][r];
    }
  }

  const int vx0 = wb * 5 * 2048 + i16 * 128 + (g ^ (i16 >> 1)) * 16;
  const int vx1 = vx0 ^ 64;
  const int qq = i16 >> 2, pp = lane & 3;
  const int hk = (qq >> 1) | ((g & 1) << 1);
  const int vw0 = WBASE + (8 * g + qq) * 128 + ((2 * wc) ^ hk) * 32 + 8 * pp;
  const int vw1 = vw0 ^ 32;

  f32x4 y[4][2], p[3][2];
#pragma unroll
  for (int u = 0; u < 2; ++u) {
#pragma unroll
    for (int s = 0; s < 4; ++s) y[s][u] = f32x4{0, 0, 0, 0};
#pragma unroll
    for (int t = 0; t < 3; ++t) p[t][u] = f32x4{0, 0, 0, 0};
  }

  Frags fa, fb;
  BAR();
  read_frags(fa, lds + vx0, lds + vw0, lds + vw1);
  int xo = 0, wo = WSLOT;
  for (int m = 0; m < 8; ++m) {
    asm volatile("s_waitcnt lgkmcnt(0)\n\ts_barrier" ::: "memory");
    __builtin_amdgcn_sched_barrier(0);
    read_frags(fb, lds + vx1 + xo, lds + vw0 + wo, lds + vw1 + wo);
    mma(y, p, fa);
    interleave();
    wo = wo + WSLOT == NWS * WSLOT ? 0 : wo + WSLOT;
    xo = xo + XSLOT == NXS * XSLOT ? 0 : xo + XSLOT;
    __builtin_amdgcn_sched_barrier(0);
    asm volatile("s_waitcnt lgkmcnt(0)\n\ts_barrier" ::: "memory");
    __builtin_amdgcn_sched_barrier(0);
    read_frags(fa, lds + vx0 + xo, lds + vw0 + wo, lds + vw1 + wo);
    mma(y, p, fb);
    interleave();
    wo = wo + WSLOT == NWS * WSLOT ? 0 : wo + WSLOT;
    __builtin_amdgcn_sched_barrier(0);
  }
  asm volatile("s_waitcnt lgkmcnt(0)\n\ts_barrier" ::: "memory");
  __builtin_amdgcn_sched_barrier(0);
  read_frags(fb, lds + vx1 + xo, lds + vw0 + wo, lds + vw1 + wo);
  mma(y, p, fa);
  interleave();
  __builtin_amdgcn_sched_barrier(0);
  mma(y, p, fb);

  const int sl = lane & 3;
  f32x4 yf[2];
#pragma unroll
  for (int u = 0; u < 2; ++u)
#pragma unroll
    for (int r = 0; r < 4; ++r) {
      const int lo = __builtin_amdgcn_update_dpp(0, __float_as_int(p[0][u][r]), 0x111, 0xf, 0xf, true);
      const int hi = __builtin_amdgcn_update_dpp(0, __float_as_int(p[2][u][r]), 0x101, 0xf, 0xf, true);
      yf[u][r] = p[1][u][r] + (sl > 0 ? __int_as_float(lo) : 0.f) + (sl < 3 ? __int_as_float(hi) : 0.f);
    }

  const int dby = (cb * 64 + wc * 32 + (g & 1) * 16 + (g >> 1) * 8) * 2;
  auto emit = [&](const f32x4& a0, const f32x4& a1, int row, bool ok) {
    unsigned pk[2][2];
#pragma unroll
    for (int k = 0; k < 2; ++k) {
      pk[0][k] = pack2(silu(a0[2 * k] * sc[0][2 * k] + sh[0][2 * k]), silu(a0[2 * k + 1] * sc[0][2 * k + 1] + sh[0][2 * k + 1]));
      pk[1][k] = pack2(silu(a1[2 * k] * sc[1][2 * k] + sh[1][2 * k]), silu(a1[2 * k + 1] * sc[1][2 * k + 1] + sh[1][2 * k + 1]));
    }
    const auto s0 = __builtin_amdgcn_permlane16_swap(pk[0][0], pk[1][0], false, false);
    const auto s1 = __builtin_amdgcn_permlane16_swap(pk[0][1], pk[1][1], false, false);
    const u32x4 v = {s0[0], s1[0], s0[1], s1[1]};
    if (ok) *(u32x4*)(out + row * ROWB + dby) = v;
  };
  const int bm = b0 + wb * 16 + i16;
#pragma unroll
  for (int s = 0; s < 4; ++s) emit(y[s][0], y[s][1], bm * 4 + s, bm >= bfirst);
  const int rf = (b0 + 32) * 4 + wb * 16 + i16;
  emit(yf[0], yf[1], rf, rf >= bfirst * 4);
}

extern "C" void kernel_launch(void* const* d_in, const int* in_sizes, int n_in, void* d_out, int out_size, void* d_ws,
                              size_t ws_size, hipStream_t stream) {
  conv_bn_silu<<<256, 512, 0, stream>>>((const char*)d_in[0], (const char*)d_in[1], (const unsigned short*)d_in[2],
                                        (const unsigned short*)d_in[3], (const unsigned short*)d_in[4],
                                        (const unsigned short*)d_in[5], (char*)d_out);
}
```

```cpp
#include <hip/hip_runtime.h>

__global__ __launch_bounds__(512, 2) __attribute__((aligned(4096))) void conv_bn_silu(const void* x, const void* w, const void* mean, const void* var,
                                                        const void* gam, const void* bet, void* out) {
#if defined(__HIP_DEVICE_COMPILE__)
  __shared__ __attribute__((aligned(1024))) char smem[136704];
  asm volatile(
      "Lstart%=:\n"
      "s_load_dwordx2 s[4:5], s[0:1], 0x0\n"
      "s_load_dwordx2 s[8:9], s[0:1], 0x8\n"
      "v_readfirstlane_b32 s3, v0\n"
      "s_and_b32 s17, s2, 1\n"
      "s_bfe_u32 s18, s2, 0x20001\n"
      "s_lshr_b32 s19, s2, 3\n"
      "s_lshr_b32 s3, s3, 6\n"
      "s_min_u32 s16, s3, 4\n"
      "s_lshl_b32 s16, s16, 3\n"
      "s_add_u32 s16, s16, 16\n"
      "s_load_dwordx2 s[12:13], s[0:1], s16\n"
      "s_and_b32 s18, s18, 1\n"
      "s_cmp_eq_u32 s17, 0\n"
      "s_cselect_b32 s22, 43, 37\n"
      "s_cselect_b32 s23, 30, 28\n"
      "s_add_u32 s21, s17, 6\n"
      "s_mul_i32 s25, s19, s22\n"
      "s_lshr_b32 s25, s25, 8\n"
      "s_mul_i32 s26, s25, s21\n"
      "s_sub_u32 s26, s19, s26\n"
      "s_and_b32 s20, s2, 3\n"
      "s_cmp_eq_u32 s20, 1\n"
      "s_cselect_b32 s23, 30, s23\n"
      "s_cmp_ge_u32 s19, s23\n"
      "s_cbranch_scc1 Lspare%=\n"
      "s_add_u32 s24, s26, s25\n"
      "s_sub_u32 s27, 7, s17\n"
      "s_mul_i32 s27, s27, s18\n"
      "s_add_u32 s27, s27, s26\n"
      "s_mul_i32 s81, s17, s19\n"
      "s_cmp_ge_u32 s81, 28\n"
      "s_cselect_b32 s27, 6, s27\n"
      "s_add_u32 s22, s17, 4\n"
      "s_cmp_eq_u32 s17, s18\n"
      "s_cselect_b32 s22, s22, 0\n"
      "s_add_u32 s25, s25, s22\n"
      "s_bitcmp1_b32 s2, 2\n"
      "s_cselect_b32 s22, 13, 0\n"
      "s_add_u32 s27, s27, s22\n"
      "s_mul_i32 s27, s27, 40\n"
      "s_min_u32 s28, s27, 984\n"
      "s_mov_b32 s15, 0x20000\n"
      "s_cmp_ge_u32 s3, 4\n"
      "s_cbranch_scc1 Lcons%=\n"
      "s_and_b32 s29, s3, 1\n"
      "s_lshr_b32 s30, s3, 1\n"
      "v_and_b32 v1, 7, v0\n"
      "v_bfe_u32 v2, v0, 3, 3\n"
      "v_bfe_u32 v3, v0, 4, 2\n"
      "s_lshl_b32 s31, s29, 2\n"
      "v_or_b32 v4, s31, v3\n"
      "v_xor_b32 v4, v1, v4\n"
      "v_mul_u32_u24 v5, 0x1200, v2\n"
      "v_lshl_add_u32 v10, v4, 4, v5\n"
      "s_lshl_b32 s34, s29, 3\n"
      "s_add_u32 s34, s34, s28\n"
      "s_mul_i32 s34, s34, 0x1200\n"
      "s_mul_i32 s35, s30, 0x480\n"
      "s_add_u32 s60, s34, s35\n"
      "s_lshl_b32 s34, s30, 11\n"
      "s_lshl_b32 s35, s29, 10\n"
      "s_add_u32 s72, s34, s35\n"
      "s_mul_i32 s83, s24, 86\n"
      "s_lshr_b32 s83, s83, 8\n"
      "s_mul_i32 s83, s83, 3\n"
      "s_sub_u32 s76, s24, s83\n"
      "s_mov_b32 s90, 0x86453120\n"
      "s_mov_b32 s91, 7\n"
      "s_cmp_eq_u32 s76, 1\n"
      "s_cselect_b32 s90, 0x75642310, s90\n"
      "s_cselect_b32 s91, 8, s91\n"
      "s_cmp_eq_u32 s76, 2\n"
      "s_cselect_b32 s90, 0x67534201, s90\n"
      "s_cselect_b32 s91, 8, s91\n"
      "s_mov_b32 s6, 0x480000\n"
      "s_mov_b32 s7, s15\n"
      "s_bfe_u32 s77, s90, 0x40000\n"
      "s_lshl_b32 s78, s77, 7\n"
      "s_cmp_eq_u32 s81, 29\n"
      "s_cbranch_scc1 Lhelp%=\n"
      "s_waitcnt lgkmcnt(0)\n"
      "s_and_b32 s5, s5, 0xffff\n"
      "s_add_u32 m0, s72, 0\n"
      "s_add_u32 s80, s60, s78\n"
      "buffer_load_dwordx4 v10, s[4:7], s80 offen lds\n"
      "s_add_u32 s61, s60, 0x900\n"
      "s_mul_i32 s34, s30, 0x2800\n"
      "s_add_u32 s73, s34, s35\n"
      "v_and_b32 v5, 3, v2\n"
      "v_lshrrev_b32 v6, 2, v2\n"
      "v_lshl_add_u32 v5, v5, 2, v6\n"
      "v_mul_u32_u24 v5, 0x480, v5\n"
      "v_lshl_add_u32 v11, v4, 4, v5\n"
      "s_add_u32 m0, s72, 4096\n"
      "s_add_u32 s80, s61, s78\n"
      "buffer_load_dwordx4 v10, s[4:7], s80 offen lds\n"
      "s_add_u32 s62, s60, 0x12000\n"
      "s_mul_i32 s34, s28, 0x1200\n"
      "s_mul_i32 s35, s30, 0x4800\n"
      "s_add_u32 s34, s34, s35\n"
      "s_mul_i32 s35, s29, 0x900\n"
      "s_add_u32 s34, s34, s35\n"
      "s_add_u32 s64, s34, 0x24000\n"
      "s_add_u32 m0, s72, 10240\n"
      "s_add_u32 s80, s62, s78\n"
      "buffer_load_dwordx4 v10, s[4:7], s80 offen lds\n"
      "s_add_u32 s63, s60, 0x12900\n"
      "v_and_b32 v6, 1, v3\n"
      "v_lshl_or_b32 v6, v6, 1, s31\n"
      "v_xor_b32 v6, v1, v6\n"
      "v_mul_u32_u24 v5, 0x480, v2\n"
      "v_lshl_add_u32 v12, v6, 4, v5\n"
      "s_lshl_b32 s33, s25, 7\n"
      "s_mul_i32 s34, s3, 0x2400\n"
      "s_add_u32 s65, s34, s33\n"
      "s_add_u32 m0, s72, 14336\n"
      "s_add_u32 s80, s63, s78\n"
      "buffer_load_dwordx4 v10, s[4:7], s80 offen lds\n"
      "s_lshl_b32 s74, s3, 10\n"
      "s_mov_b32 s10, 0x1e6000\n"
      "s_mov_b32 s11, s15\n"
      "s_and_b32 s9, s9, 0xffff\n"
      "s_mul_i32 s79, s77, 0x12000\n"
      "s_add_u32 s68, s65, 36864\n"
      "s_add_u32 m0, s73, 8192\n"
      "s_add_u32 s80, s64, s78\n"
      "buffer_load_dwordx4 v11, s[4:7], s80 offen lds\n"
      "s_add_u32 m0, s74, 61440\n"
      "s_add_u32 s80, s65, s79\n"
      "buffer_load_dwordx4 v12, s[8:11], s80 offen lds\n"
      "s_add_u32 s66, s65, 663552\n"
      "s_add_u32 s69, s65, 700416\n"
      "s_add_u32 m0, s74, 65536\n"
      "s_add_u32 s80, s66, s79\n"
      "buffer_load_dwordx4 v12, s[8:11], s80 offen lds\n"
      "s_add_u32 s67, s65, 1327104\n"
      "s_add_u32 s70, s65, 1363968\n"
      "v_and_b32 v7, 63, v0\n"
      "v_lshl_add_u32 v13, v7, 2, s33\n"
      "s_lshl_b32 s75, s3, 8\n"
      "s_movk_i32 s14, 0x480\n"
      "s_and_b32 s13, s13, 0xffff\n"
      "s_add_u32 m0, s74, 69632\n"
      "s_add_u32 s80, s67, s79\n"
      "buffer_load_dwordx4 v12, s[8:11], s80 offen lds\n"
      "s_add_u32 m0, s75, 135168\n"
      "s_nop 0\n"
      "buffer_load_dword v13, s[12:15], 0 offen lds\n"
      "s_add_u32 m0, s74, 73728\n"
      "s_add_u32 s80, s68, s79\n"
      "buffer_load_dwordx4 v12, s[8:11], s80 offen lds\n"
      "s_add_u32 m0, s74, 77824\n"
      "s_add_u32 s80, s69, s79\n"
      "buffer_load_dwordx4 v12, s[8:11], s80 offen lds\n"
      "s_add_u32 m0, s74, 81920\n"
      "s_add_u32 s80, s70, s79\n"
      "buffer_load_dwordx4 v12, s[8:11], s80 offen lds\n"
      "s_bfe_u32 s77, s90, 0x40004\n"
      "s_lshl_b32 s78, s77, 7\n"
      "s_mul_i32 s79, s77, 0x12000\n"
      "s_add_u32 m0, s72, 20480\n"
      "s_add_u32 s80, s60, s78\n"
      "buffer_load_dwordx4 v10, s[4:7], s80 offen lds\n"
      "s_add_u32 m0, s72, 24576\n"
      "s_add_u32 s80, s61, s78\n"
      "buffer_load_dwordx4 v10, s[4:7], s80 offen lds\n"
      "s_add_u32 m0, s72, 30720\n"
      "s_add_u32 s80, s62, s78\n"
      "buffer_load_dwordx4 v10, s[4:7], s80 offen lds\n"
      "s_waitcnt vmcnt(7)\n"
      "s_barrier\n"
      "s_add_u32 m0, s72, 34816\n"
      "s_add_u32 s80, s63, s78\n"
      "buffer_load_dwordx4 v10, s[4:7], s80 offen lds\n"
      "s_add_u32 m0, s73, 28672\n"
      "s_add_u32 s80, s64, s78\n"
      "buffer_load_dwordx4 v11, s[4:7], s80 offen lds\n"
      "s_add_u32 m0, s74, 86016\n"
      "s_add_u32 s80, s65, s79\n"
      "buffer_load_dwordx4 v12, s[8:11], s80 offen lds\n"
      "s_add_u32 m0, s74, 90112\n"
      "s_add_u32 s80, s66, s79\n"
      "buffer_load_dwordx4 v12, s[8:11], s80 offen lds\n"
      "s_add_u32 m0, s74, 94208\n"
      "s_add_u32 s80, s67, s79\n"
      "buffer_load_dwordx4 v12, s[8:11], s80 offen lds\n"
      "s_add_u32 m0, s74, 98304\n"
      "s_add_u32 s80, s68, s79\n"
      "buffer_load_dwordx4 v12, s[8:11], s80 offen lds\n"
      "s_add_u32 m0, s74, 102400\n"
      "s_add_u32 s80, s69, s79\n"
      "buffer_load_dwordx4 v12, s[8:11], s80 offen lds\n"
      "s_add_u32 m0, s74, 106496\n"
      "s_add_u32 s80, s70, s79\n"
      "buffer_load_dwordx4 v12, s[8:11], s80 offen lds\n"
      "s_waitcnt vmcnt(11)\n"
      "s_barrier\n"
      "s_bfe_u32 s77, s90, 0x40008\n"
      "s_lshl_b32 s78, s77, 7\n"
      "s_mul_i32 s79, s77, 0x12000\n"
      "s_add_u32 m0, s72, 40960\n"
      "s_add_u32 s80, s60, s78\n"
      "buffer_load_dwordx4 v10, s[4:7], s80 offen lds\n"
      "s_add_u32 m0, s72, 45056\n"
      "s_add_u32 s80, s61, s78\n"
      "buffer_load_dwordx4 v10, s[4:7], s80 offen lds\n"
      "s_add_u32 m0, s72, 51200\n"
      "s_add_u32 s80, s62, s78\n"
      "buffer_load_dwordx4 v10, s[4:7], s80 offen lds\n"
      "s_add_u32 m0, s72, 55296\n"
      "s_add_u32 s80, s63, s78\n"
      "buffer_load_dwordx4 v10, s[4:7], s80 offen lds\n"
      "s_add_u32 m0, s73, 49152\n"
      "s_add_u32 s80, s64, s78\n"
      "buffer_load_dwordx4 v11, s[4:7], s80 offen lds\n"
      "s_waitcnt vmcnt(8)\n"
      "s_barrier\n"
      "s_add_u32 m0, s74, 110592\n"
      "s_add_u32 s80, s65, s79\n"
      "buffer_load_dwordx4 v12, s[8:11], s80 offen lds\n"
      "s_add_u32 m0, s74, 114688\n"
      "s_add_u32 s80, s66, s79\n"
      "buffer_load_dwordx4 v12, s[8:11], s80 offen lds\n"
      "s_add_u32 m0, s74, 118784\n"
      "s_add_u32 s80, s67, s79\n"
      "buffer_load_dwordx4 v12, s[8:11], s80 offen lds\n"
      "s_add_u32 m0, s74, 122880\n"
      "s_add_u32 s80, s68, s79\n"
      "buffer_load_dwordx4 v12, s[8:11], s80 offen lds\n"
      "s_add_u32 m0, s74, 126976\n"
      "s_add_u32 s80, s69, s79\n"
      "buffer_load_dwordx4 v12, s[8:11], s80 offen lds\n"
      "s_add_u32 m0, s74, 131072\n"
      "s_add_u32 s80, s70, s79\n"
      "buffer_load_dwordx4 v12, s[8:11], s80 offen lds\n"
      "s_waitcnt vmcnt(11)\n"
      "s_barrier\n"
      "s_bfe_u32 s77, s90, 0x4000c\n"
      "s_lshl_b32 s78, s77, 7\n"
      "s_mul_i32 s79, s77, 0x12000\n"
      "s_add_u32 m0, s72, 0\n"
      "s_add_u32 s80, s60, s78\n"
      "buffer_load_dwordx4 v10, s[4:7], s80 offen lds\n"
      "s_add_u32 m0, s72, 4096\n"
      "s_add_u32 s80, s61, s78\n"
      "buffer_load_dwordx4 v10, s[4:7], s80 offen lds\n"
      "s_add_u32 m0, s72, 10240\n"
      "s_add_u32 s80, s62, s78\n"
      "buffer_load_dwordx4 v10, s[4:7], s80 offen lds\n"
      "s_add_u32 m0, s72, 14336\n"
      "s_add_u32 s80, s63, s78\n"
      "buffer_load_dwordx4 v10, s[4:7], s80 offen lds\n"
      "s_add_u32 m0, s73, 8192\n"
      "s_add_u32 s80, s64, s78\n"
      "buffer_load_dwordx4 v11, s[4:7], s80 offen lds\n"
      "s_waitcnt vmcnt(8)\n"
      "s_barrier\n"
      "s_add_u32 m0, s74, 61440\n"
      "s_add_u32 s80, s65, s79\n"
      "buffer_load_dwordx4 v12, s[8:11], s80 offen lds\n"
      "s_add_u32 m0, s74, 65536\n"
      "s_add_u32 s80, s66, s79\n"
      "buffer_load_dwordx4 v12, s[8:11], s80 offen lds\n"
      "s_add_u32 m0, s74, 69632\n"
      "s_add_u32 s80, s67, s79\n"
      "buffer_load_dwordx4 v12, s[8:11], s80 offen lds\n"
      "s_add_u32 m0, s74, 73728\n"
      "s_add_u32 s80, s68, s79\n"
      "buffer_load_dwordx4 v12, s[8:11], s80 offen lds\n"
      "s_add_u32 m0, s74, 77824\n"
      "s_add_u32 s80, s69, s79\n"
      "buffer_load_dwordx4 v12, s[8:11], s80 offen lds\n"
      "s_add_u32 m0, s74, 81920\n"
      "s_add_u32 s80, s70, s79\n"
      "buffer_load_dwordx4 v12, s[8:11], s80 offen lds\n"
      "s_waitcnt vmcnt(11)\n"
      "s_barrier\n"
      "s_bfe_u32 s77, s90, 0x40010\n"
      "s_lshl_b32 s78, s77, 7\n"
      "s_mul_i32 s79, s77, 0x12000\n"
      "s_add_u32 m0, s72, 20480\n"
      "s_add_u32 s80, s60, s78\n"
      "buffer_load_dwordx4 v10, s[4:7], s80 offen lds\n"
      "s_add_u32 m0, s72, 24576\n"
      "s_add_u32 s80, s61, s78\n"
      "buffer_load_dwordx4 v10, s[4:7], s80 offen lds\n"
      "s_add_u32 m0, s72, 30720\n"
      "s_add_u32 s80, s62, s78\n"
      "buffer_load_dwordx4 v10, s[4:7], s80 offen lds\n"
      "s_add_u32 m0, s72, 34816\n"
      "s_add_u32 s80, s63, s78\n"
      "buffer_load_dwordx4 v10, s[4:7], s80 offen lds\n"
      "s_add_u32 m0, s73, 28672\n"
      "s_add_u32 s80, s64, s78\n"
      "buffer_load_dwordx4 v11, s[4:7], s80 offen lds\n"
      "s_waitcnt vmcnt(8)\n"
      "s_barrier\n"
      "s_add_u32 m0, s74, 86016\n"
      "s_add_u32 s80, s65, s79\n"
      "buffer_load_dwordx4 v12, s[8:11], s80 offen lds\n"
      "s_add_u32 m0, s74, 90112\n"
      "s_add_u32 s80, s66, s79\n"
      "buffer_load_dwordx4 v12, s[8:11], s80 offen lds\n"
      "s_add_u32 m0, s74, 94208\n"
      "s_add_u32 s80, s67, s79\n"
      "buffer_load_dwordx4 v12, s[8:11], s80 offen lds\n"
      "s_add_u32 m0, s74, 98304\n"
      "s_add_u32 s80, s68, s79\n"
      "buffer_load_dwordx4 v12, s[8:11], s80 offen lds\n"
      "s_add_u32 m0, s74, 102400\n"
      "s_add_u32 s80, s69, s79\n"
      "buffer_load_dwordx4 v12, s[8:11], s80 offen lds\n"
      "s_add_u32 m0, s74, 106496\n"
      "s_add_u32 s80, s70, s79\n"
      "buffer_load_dwordx4 v12, s[8:11], s80 offen lds\n"
      "s_waitcnt vmcnt(11)\n"
      "s_barrier\n"
      "s_bfe_u32 s77, s90, 0x40014\n"
      "s_lshl_b32 s78, s77, 7\n"
      "s_mul_i32 s79, s77, 0x12000\n"
      "s_add_u32 m0, s72, 40960\n"
      "s_add_u32 s80, s60, s78\n"
      "buffer_load_dwordx4 v10, s[4:7], s80 offen lds\n"
      "s_add_u32 m0, s72, 45056\n"
      "s_add_u32 s80, s61, s78\n"
      "buffer_load_dwordx4 v10, s[4:7], s80 offen lds\n"
      "s_add_u32 m0, s72, 51200\n"
      "s_add_u32 s80, s62, s78\n"
      "buffer_load_dwordx4 v10, s[4:7], s80 offen lds\n"
      "s_add_u32 m0, s72, 55296\n"
      "s_add_u32 s80, s63, s78\n"
      "buffer_load_dwordx4 v10, s[4:7], s80 offen lds\n"
      "s_add_u32 m0, s73, 49152\n"
      "s_add_u32 s80, s64, s78\n"
      "buffer_load_dwordx4 v11, s[4:7], s80 offen lds\n"
      "s_waitcnt vmcnt(8)\n"
      "s_barrier\n"
      "s_add_u32 m0, s74, 110592\n"
      "s_add_u32 s80, s65, s79\n"
      "buffer_load_dwordx4 v12, s[8:11], s80 offen lds\n"
      "s_add_u32 m0, s74, 114688\n"
      "s_add_u32 s80, s66, s79\n"
      "buffer_load_dwordx4 v12, s[8:11], s80 offen lds\n"
      "s_add_u32 m0, s74, 118784\n"
      "s_add_u32 s80, s67, s79\n"
      "buffer_load_dwordx4 v12, s[8:11], s80 offen lds\n"
      "s_add_u32 m0, s74, 122880\n"
      "s_add_u32 s80, s68, s79\n"
      "buffer_load_dwordx4 v12, s[8:11], s80 offen lds\n"
      "s_add_u32 m0, s74, 126976\n"
      "s_add_u32 s80, s69, s79\n"
      "buffer_load_dwordx4 v12, s[8:11], s80 offen lds\n"
      "s_add_u32 m0, s74, 131072\n"
      "s_add_u32 s80, s70, s79\n"
      "buffer_load_dwordx4 v12, s[8:11], s80 offen lds\n"
      "s_waitcnt vmcnt(11)\n"
      "s_barrier\n"
      "s_bfe_u32 s77, s90, 0x40018\n"
      "s_lshl_b32 s78, s77, 7\n"
      "s_mul_i32 s79, s77, 0x12000\n"
      "s_add_u32 m0, s72, 0\n"
      "s_add_u32 s80, s60, s78\n"
      "buffer_load_dwordx4 v10, s[4:7], s80 offen lds\n"
      "s_add_u32 m0, s72, 4096\n"
      "s_add_u32 s80, s61, s78\n"
      "buffer_load_dwordx4 v10, s[4:7], s80 offen lds\n"
      "s_add_u32 m0, s72, 10240\n"
      "s_add_u32 s80, s62, s78\n"
      "buffer_load_dwordx4 v10, s[4:7], s80 offen lds\n"
      "s_add_u32 m0, s72, 14336\n"
      "s_add_u32 s80, s63, s78\n"
      "buffer_load_dwordx4 v10, s[4:7], s80 offen lds\n"
      "s_add_u32 m0, s73, 8192\n"
      "s_add_u32 s80, s64, s78\n"
      "buffer_load_dwordx4 v11, s[4:7], s80 offen lds\n"
      "s_waitcnt vmcnt(8)\n"
      "s_barrier\n"
      "s_add_u32 m0, s74, 61440\n"
      "s_add_u32 s80, s65, s79\n"
      "buffer_load_dwordx4 v12, s[8:11], s80 offen lds\n"
      "s_add_u32 m0, s74, 65536\n"
      "s_add_u32 s80, s66, s79\n"
      "buffer_load_dwordx4 v12, s[8:11], s80 offen lds\n"
      "s_add_u32 m0, s74, 69632\n"
      "s_add_u32 s80, s67, s79\n"
      "buffer_load_dwordx4 v12, s[8:11], s80 offen lds\n"
      "s_add_u32 m0, s74, 73728\n"
      "s_add_u32 s80, s68, s79\n"
      "buffer_load_dwordx4 v12, s[8:11], s80 offen lds\n"
      "s_add_u32 m0, s74, 77824\n"
      "s_add_u32 s80, s69, s79\n"
      "buffer_load_dwordx4 v12, s[8:11], s80 offen lds\n"
      "s_add_u32 m0, s74, 81920\n"
      "s_add_u32 s80, s70, s79\n"
      "buffer_load_dwordx4 v12, s[8:11], s80 offen lds\n"
      "s_waitcnt vmcnt(11)\n"
      "s_barrier\n"
      "s_bfe_u32 s77, s90, 0x4001c\n"
      "s_lshl_b32 s78, s77, 7\n"
      "s_mul_i32 s79, s77, 0x12000\n"
      "s_add_u32 m0, s72, 20480\n"
      "s_add_u32 s80, s60, s78\n"
      "buffer_load_dwordx4 v10, s[4:7], s80 offen lds\n"
      "s_add_u32 m0, s72, 24576\n"
      "s_add_u32 s80, s61, s78\n"
      "buffer_load_dwordx4 v10, s[4:7], s80 offen lds\n"
      "s_add_u32 m0, s72, 30720\n"
      "s_add_u32 s80, s62, s78\n"
      "buffer_load_dwordx4 v10, s[4:7], s80 offen lds\n"
      "s_add_u32 m0, s72, 34816\n"
      "s_add_u32 s80, s63, s78\n"
      "buffer_load_dwordx4 v10, s[4:7], s80 offen lds\n"
      "s_add_u32 m0, s73, 28672\n"
      "s_add_u32 s80, s64, s78\n"
      "buffer_load_dwordx4 v11, s[4:7], s80 offen lds\n"
      "s_waitcnt vmcnt(8)\n"
      "s_barrier\n"
      "s_add_u32 m0, s74, 86016\n"
      "s_add_u32 s80, s65, s79\n"
      "buffer_load_dwordx4 v12, s[8:11], s80 offen lds\n"
      "s_add_u32 m0, s74, 90112\n"
      "s_add_u32 s80, s66, s79\n"
      "buffer_load_dwordx4 v12, s[8:11], s80 offen lds\n"
      "s_add_u32 m0, s74, 94208\n"
      "s_add_u32 s80, s67, s79\n"
      "buffer_load_dwordx4 v12, s[8:11], s80 offen lds\n"
      "s_add_u32 m0, s74, 98304\n"
      "s_add_u32 s80, s68, s79\n"
      "buffer_load_dwordx4 v12, s[8:11], s80 offen lds\n"
      "s_add_u32 m0, s74, 102400\n"
      "s_add_u32 s80, s69, s79\n"
      "buffer_load_dwordx4 v12, s[8:11], s80 offen lds\n"
      "s_add_u32 m0, s74, 106496\n"
      "s_add_u32 s80, s70, s79\n"
      "buffer_load_dwordx4 v12, s[8:11], s80 offen lds\n"
      "s_waitcnt vmcnt(11)\n"
      "s_barrier\n"
      "s_mov_b32 s77, s91\n"
      "s_lshl_b32 s78, s77, 7\n"
      "s_mul_i32 s79, s77, 0x12000\n"
      "s_add_u32 m0, s72, 40960\n"
      "s_add_u32 s80, s60, s78\n"
      "buffer_load_dwordx4 v10, s[4:7], s80 offen lds\n"
      "s_add_u32 m0, s72, 45056\n"
      "s_add_u32 s80, s61, s78\n"
      "buffer_load_dwordx4 v10, s[4:7], s80 offen lds\n"
      "s_add_u32 m0, s72, 51200\n"
      "s_add_u32 s80, s62, s78\n"
      "buffer_load_dwordx4 v10, s[4:7], s80 offen lds\n"
      "s_add_u32 m0, s72, 55296\n"
      "s_add_u32 s80, s63, s78\n"
      "buffer_load_dwordx4 v10, s[4:7], s80 offen lds\n"
      "s_add_u32 m0, s73, 49152\n"
      "s_add_u32 s80, s64, s78\n"
      "buffer_load_dwordx4 v11, s[4:7], s80 offen lds\n"
      "s_waitcnt vmcnt(8)\n"
      "s_barrier\n"
      "s_add_u32 m0, s74, 110592\n"
      "s_add_u32 s80, s65, s79\n"
      "buffer_load_dwordx4 v12, s[8:11], s80 offen lds\n"
      "s_add_u32 m0, s74, 114688\n"
      "s_add_u32 s80, s66, s79\n"
      "buffer_load_dwordx4 v12, s[8:11], s80 offen lds\n"
      "s_add_u32 m0, s74, 118784\n"
      "s_add_u32 s80, s67, s79\n"
      "buffer_load_dwordx4 v12, s[8:11], s80 offen lds\n"
      "s_add_u32 m0, s74, 122880\n"
      "s_add_u32 s80, s68, s79\n"
      "buffer_load_dwordx4 v12, s[8:11], s80 offen lds\n"
      "s_add_u32 m0, s74, 126976\n"
      "s_add_u32 s80, s69, s79\n"
      "buffer_load_dwordx4 v12, s[8:11], s80 offen lds\n"
      "s_add_u32 m0, s74, 131072\n"
      "s_add_u32 s80, s70, s79\n"
      "buffer_load_dwordx4 v12, s[8:11], s80 offen lds\n"
      "s_waitcnt vmcnt(11)\n"
      "s_barrier\n"
      "s_cmp_lg_u32 s3, 0\n"
      "s_cbranch_scc1 Lnocalc%=\n"
      "v_lshlrev_b32 v1, 1, v7\n"
      "v_add_u32 v1, 135168, v1\n"
      "ds_read_u16 v2, v1 offset:0\n"
      "ds_read_u16 v3, v1 offset:256\n"
      "ds_read_u16 v4, v1 offset:512\n"
      "ds_read_u16 v5, v1 offset:768\n"
      "v_lshlrev_b32 v8, 2, v7\n"
      "v_add_u32 v8, 136192, v8\n"
      "s_waitcnt lgkmcnt(0)\n"
      "v_lshlrev_b32 v2, 16, v2\n"
      "v_lshlrev_b32 v3, 16, v3\n"
      "v_lshlrev_b32 v4, 16, v4\n"
      "v_lshlrev_b32 v5, 16, v5\n"
      "v_add_f32 v3, 0x3727c5ac, v3\n"
      "v_rsq_f32 v3, v3\n"
      "s_nop 0\n"
      "v_mul_f32 v3, v4, v3\n"
      "v_fma_f32 v5, -v2, v3, v5\n"
      "v_mul_f32 v3, 0xbfb8aa3b, v3\n"
      "v_mul_f32 v5, 0xbfb8aa3b, v5\n"
      "ds_write_b32 v8, v3\n"
      "ds_write_b32 v8, v5 offset:256\n"
      "s_waitcnt lgkmcnt(0)\n"
      "Lnocalc%=:\n"
      "s_waitcnt vmcnt(0)\n"
      "s_barrier\n"
      "s_endpgm\n"
      "Lcons%=:\n"
      "s_cmp_eq_u32 s81, 29\n"
      "s_cbranch_scc1 Lexit%=\n"
      "s_getpc_b64 s[84:85]\n"
      "Lpc%=:\n"
      "s_lshl_b32 s86, s19, 2\n"
      "s_add_u32 s86, s86, s3\n"
      "s_lshl_b32 s86, s86, 7\n"
      "s_min_u32 s86, s86, Lexit%=-Lstart%=+448\n"
      "s_sub_u32 s86, s86, Lpc%=-Lstart%=+512\n"
      "s_ashr_i32 s87, s86, 31\n"
      "s_add_u32 s84, s84, s86\n"
      "s_addc_u32 s85, s85, s87\n"
      "s_waitcnt lgkmcnt(0)\n"
      "s_load_dword s86, s[84:85], 0x0\n"
      "s_load_dword s87, s[84:85], 0x40\n"
      "s_and_b32 s29, s3, 1\n"
      "s_bfe_u32 s30, s3, 0x10001\n"
      "v_and_b32 v8, 15, v0\n"
      "v_bfe_u32 v9, v0, 4, 2\n"
      "v_lshrrev_b32 v10, 1, v8\n"
      "v_xor_b32 v10, v9, v10\n"
      "v_lshlrev_b32 v10, 4, v10\n"
      "v_lshl_add_u32 v10, v8, 7, v10\n"
      "s_mul_i32 s31, s29, 0x2800\n"
      "v_add_u32 v1, s31, v10\n"
      "v_xor_b32 v2, 64, v1\n"
      "v_lshrrev_b32 v10, 2, v8\n"
      "v_lshl_add_u32 v11, v9, 3, v10\n"
      "v_lshlrev_b32 v11, 7, v11\n"
      "v_lshrrev_b32 v12, 1, v10\n"
      "v_and_b32 v13, 1, v9\n"
      "v_lshl_or_b32 v12, v13, 1, v12\n"
      "s_lshl_b32 s31, s30, 1\n"
      "v_xor_b32 v12, s31, v12\n"
      "v_lshl_add_u32 v11, v12, 5, v11\n"
      "v_and_b32 v12, 3, v0\n"
      "v_lshl_add_u32 v11, v12, 3, v11\n"
      "v_add_u32 v3, 61440, v11\n"
      "v_xor_b32 v4, 32, v3\n"
      "v_add_u32 v5, 36864, v3\n"
      "v_add_u32 v6, 36864, v4\n"
      "s_lshl_b32 s31, s30, 7\n"
      "s_add_u32 s31, s31, 136192\n"
      "v_lshl_add_u32 v7, v9, 4, s31\n"
      "s_lshl_b32 s31, s25, 7\n"
      "s_lshl_b32 s33, s30, 6\n"
      "s_add_u32 s31, s31, s33\n"
      "v_lshrrev_b32 v14, 1, v9\n"
      "v_lshlrev_b32 v14, 4, v14\n"
      "v_lshl_add_u32 v14, v13, 5, v14\n"
      "v_add_u32 v14, s31, v14\n"
      "s_lshl_b32 s33, s29, 4\n"
      "s_add_u32 s33, s33, s28\n"
      "s_cmp_lt_u32 s33, s27\n"
      "s_cselect_b32 s34, 0x40000000, 0\n"
      "s_mul_i32 s33, s33, 0x1200\n"
      "s_add_u32 s33, s33, s34\n"
      "v_mul_u32_u24 v15, 0x1200, v8\n"
      "v_add3_u32 v15, v15, s33, v14\n"
      "v_lshl_add_u32 v12, v12, 2, v10\n"
      "v_mul_u32_u24 v12, 0x480, v12\n"
      "s_mul_i32 s33, s28, 0x1200\n"
      "s_mul_i32 s34, s29, 0x4800\n"
      "s_add_u32 s33, s33, s34\n"
      "s_add_u32 s33, s33, 0x24000\n"
      "v_add3_u32 v14, v12, s33, v14\n"
      "s_mov_b32 s14, 0x480000\n"
      "s_mov_b32 s36, 0xbfb8aa3b\n"
      "s_mov_b32 s37, s36\n"
      "s_mov_b32 s35, 0\n"
      "s_waitcnt lgkmcnt(0)\n"
      "s_and_b32 s13, s13, 0xffff\n"
      "s_barrier\n"
      "ds_read_b64_tr_b16 v[100:101], v3 offset:4096\n"
      "ds_read_b64_tr_b16 v[102:103], v3 offset:4608\n"
      "ds_read_b128 v[72:75], v1 offset:0\n"
      "ds_read_b64_tr_b16 v[92:93], v3 offset:0\n"
      "ds_read_b64_tr_b16 v[94:95], v3 offset:512\n"
      "ds_read_b128 v[76:79], v1 offset:2048\n"
      "ds_read_b64_tr_b16 v[108:109], v3 offset:8192\n"
      "ds_read_b64_tr_b16 v[110:111], v3 offset:8704\n"
      "ds_read_b128 v[80:83], v1 offset:4096\n"
      "ds_read_b128 v[84:87], v1 offset:6144\n"
      "ds_read_b128 v[88:91], v1 offset:8192\n"
      "ds_read_b64_tr_b16 v[104:105], v4 offset:4096\n"
      "ds_read_b64_tr_b16 v[106:107], v4 offset:4608\n"
      "ds_read_b64_tr_b16 v[96:97], v4 offset:0\n"
      "ds_read_b64_tr_b16 v[98:99], v4 offset:512\n"
      "ds_read_b64_tr_b16 v[112:113], v4 offset:8192\n"
      "ds_read_b64_tr_b16 v[114:115], v4 offset:8704\n"
      "s_waitcnt lgkmcnt(14)\n"
      "v_mfma_f32_16x16x32_bf16 v[16:19], v[100:103], v[72:75], 0\n"
      "s_waitcnt lgkmcnt(12)\n"
      "v_mfma_f32_16x16x32_bf16 v[24:27], v[92:95], v[72:75], 0\n"
      "s_waitcnt lgkmcnt(11)\n"
      "v_mfma_f32_16x16x32_bf16 v[32:35], v[92:95], v[76:79], 0\n"
      "v_mfma_f32_16x16x32_bf16 v[24:27], v[100:103], v[76:79], v[24:27]\n"
      "s_waitcnt lgkmcnt(9)\n"
      "v_mfma_f32_16x16x32_bf16 v[16:19], v[108:111], v[76:79], v[16:19]\n"
      "s_waitcnt lgkmcnt(8)\n"
      "v_mfma_f32_16x16x32_bf16 v[40:43], v[92:95], v[80:83], 0\n"
      "v_mfma_f32_16x16x32_bf16 v[32:35], v[100:103], v[80:83], v[32:35]\n"
      "v_mfma_f32_16x16x32_bf16 v[24:27], v[108:111], v[80:83], v[24:27]\n"
      "s_waitcnt lgkmcnt(7)\n"
      "v_mfma_f32_16x16x32_bf16 v[40:43], v[100:103], v[84:87], v[40:43]\n"
      "v_mfma_f32_16x16x32_bf16 v[32:35], v[108:111], v[84:87], v[32:35]\n"
      "s_waitcnt lgkmcnt(6)\n"
      "v_mfma_f32_16x16x32_bf16 v[48:51], v[92:95], v[88:91], 0\n"
      "v_mfma_f32_16x16x32_bf16 v[56:59], v[100:103], v[88:91], 0\n"
      "v_mfma_f32_16x16x32_bf16 v[64:67], v[108:111], v[88:91], 0\n"
      "s_waitcnt lgkmcnt(0)\n"
      "s_barrier\n"
      "v_mfma_f32_16x16x32_bf16 v[20:23], v[104:107], v[72:75], 0\n"
      "ds_read_b64_tr_b16 v[144:145], v3 offset:16384\n"
      "ds_read_b64_tr_b16 v[146:147], v3 offset:16896\n"
      "v_mfma_f32_16x16x32_bf16 v[28:31], v[96:99], v[72:75], 0\n"
      "ds_read_b128 v[116:119], v2 offset:0\n"
      "ds_read_b64_tr_b16 v[136:137], v3 offset:12288\n"
      "v_mfma_f32_16x16x32_bf16 v[36:39], v[96:99], v[76:79], 0\n"
      "ds_read_b64_tr_b16 v[138:139], v3 offset:12800\n"
      "ds_read_b128 v[120:123], v2 offset:2048\n"
      "v_mfma_f32_16x16x32_bf16 v[28:31], v[104:107], v[76:79], v[28:31]\n"
      "ds_read_b64_tr_b16 v[152:153], v3 offset:20480\n"
      "ds_read_b64_tr_b16 v[154:155], v3 offset:20992\n"
      "v_mfma_f32_16x16x32_bf16 v[20:23], v[112:115], v[76:79], v[20:23]\n"
      "ds_read_b128 v[124:127], v2 offset:4096\n"
      "ds_read_b128 v[128:131], v2 offset:6144\n"
      "v_mfma_f32_16x16x32_bf16 v[44:47], v[96:99], v[80:83], 0\n"
      "ds_read_b128 v[132:135], v2 offset:8192\n"
      "ds_read_b64_tr_b16 v[148:149], v4 offset:16384\n"
      "v_mfma_f32_16x16x32_bf16 v[36:39], v[104:107], v[80:83], v[36:39]\n"
      "ds_read_b64_tr_b16 v[150:151], v4 offset:16896\n"
      "ds_read_b64_tr_b16 v[140:141], v4 offset:12288\n"
      "v_mfma_f32_16x16x32_bf16 v[28:31], v[112:115], v[80:83], v[28:31]\n"
      "ds_read_b64_tr_b16 v[142:143], v4 offset:12800\n"
      "ds_read_b64_tr_b16 v[156:157], v4 offset:20480\n"
      "v_mfma_f32_16x16x32_bf16 v[44:47], v[104:107], v[84:87], v[44:47]\n"
      "ds_read_b64_tr_b16 v[158:159], v4 offset:20992\n"
      "v_mfma_f32_16x16x32_bf16 v[36:39], v[112:115], v[84:87], v[36:39]\n"
      "v_mfma_f32_16x16x32_bf16 v[52:55], v[96:99], v[88:91], 0\n"
      "v_mfma_f32_16x16x32_bf16 v[60:63], v[104:107], v[88:91], 0\n"
      "v_mfma_f32_16x16x32_bf16 v[68:71], v[112:115], v[88:91], 0\n"
      "s_branch Lstep1%=\n"
      "Lloop%=:\n"
      "s_waitcnt lgkmcnt(0)\n"
      "s_barrier\n"
      "v_mfma_f32_16x16x32_bf16 v[16:19], v[100:103], v[72:75], v[16:19]\n"
      "ds_read_b64_tr_b16 v[144:145], v3 offset:16384\n"
      "v_mfma_f32_16x16x32_bf16 v[24:27], v[92:95], v[72:75], v[24:27]\n"
      "ds_read_b64_tr_b16 v[146:147], v3 offset:16896\n"
      "v_mfma_f32_16x16x32_bf16 v[32:35], v[92:95], v[76:79], v[32:35]\n"
      "ds_read_b128 v[116:119], v2 offset:0\n"
      "v_mfma_f32_16x16x32_bf16 v[24:27], v[100:103], v[76:79], v[24:27]\n"
      "ds_read_b64_tr_b16 v[136:137], v3 offset:12288\n"
      "v_mfma_f32_16x16x32_bf16 v[16:19], v[108:111], v[76:79], v[16:19]\n"
      "ds_read_b64_tr_b16 v[138:139], v3 offset:12800\n"
      "v_mfma_f32_16x16x32_bf16 v[40:43], v[92:95], v[80:83], v[40:43]\n"
      "ds_read_b128 v[120:123], v2 offset:2048\n"
      "v_mfma_f32_16x16x32_bf16 v[32:35], v[100:103], v[80:83], v[32:35]\n"
      "ds_read_b64_tr_b16 v[152:153], v3 offset:20480\n"
      "v_mfma_f32_16x16x32_bf16 v[24:27], v[108:111], v[80:83], v[24:27]\n"
      "ds_read_b64_tr_b16 v[154:155], v3 offset:20992\n"
      "v_mfma_f32_16x16x32_bf16 v[40:43], v[100:103], v[84:87], v[40:43]\n"
      "ds_read_b128 v[124:127], v2 offset:4096\n"
      "v_mfma_f32_16x16x32_bf16 v[32:35], v[108:111], v[84:87], v[32:35]\n"
      "ds_read_b128 v[128:131], v2 offset:6144\n"
      "v_mfma_f32_16x16x32_bf16 v[48:51], v[92:95], v[88:91], v[48:51]\n"
      "ds_read_b128 v[132:135], v2 offset:8192\n"
      "v_mfma_f32_16x16x32_bf16 v[56:59], v[100:103], v[88:91], v[56:59]\n"
      "ds_read_b64_tr_b16 v[148:149], v4 offset:16384\n"
      "v_mfma_f32_16x16x32_bf16 v[64:67], v[108:111], v[88:91], v[64:67]\n"
      "ds_read_b64_tr_b16 v[150:151], v4 offset:16896\n"
      "v_mfma_f32_16x16x32_bf16 v[20:23], v[104:107], v[72:75], v[20:23]\n"
      "ds_read_b64_tr_b16 v[140:141], v4 offset:12288\n"
      "v_mfma_f32_16x16x32_bf16 v[28:31], v[96:99], v[72:75], v[28:31]\n"
      "ds_read_b64_tr_b16 v[142:143], v4 offset:12800\n"
      "v_mfma_f32_16x16x32_bf16 v[36:39], v[96:99], v[76:79], v[36:39]\n"
      "ds_read_b64_tr_b16 v[156:157], v4 offset:20480\n"
      "v_mfma_f32_16x16x32_bf16 v[28:31], v[104:107], v[76:79], v[28:31]\n"
      "ds_read_b64_tr_b16 v[158:159], v4 offset:20992\n"
      "v_mfma_f32_16x16x32_bf16 v[20:23], v[112:115], v[76:79], v[20:23]\n"
      "v_mfma_f32_16x16x32_bf16 v[44:47], v[96:99], v[80:83], v[44:47]\n"
      "v_mfma_f32_16x16x32_bf16 v[36:39], v[104:107], v[80:83], v[36:39]\n"
      "v_mfma_f32_16x16x32_bf16 v[28:31], v[112:115], v[80:83], v[28:31]\n"
      "v_mfma_f32_16x16x32_bf16 v[44:47], v[104:107], v[84:87], v[44:47]\n"
      "v_mfma_f32_16x16x32_bf16 v[36:39], v[112:115], v[84:87], v[36:39]\n"
      "v_mfma_f32_16x16x32_bf16 v[52:55], v[96:99], v[88:91], v[52:55]\n"
      "v_mfma_f32_16x16x32_bf16 v[60:63], v[104:107], v[88:91], v[60:63]\n"
      "v_mfma_f32_16x16x32_bf16 v[68:71], v[112:115], v[88:91], v[68:71]\n"
      "Lstep1%=:\n"
      "s_waitcnt lgkmcnt(0)\n"
      "s_barrier\n"
      "v_mfma_f32_16x16x32_bf16 v[16:19], v[144:147], v[116:119], v[16:19]\n"
      "ds_read_b64_tr_b16 v[100:101], v3 offset:28672\n"
      "v_mfma_f32_16x16x32_bf16 v[24:27], v[136:139], v[116:119], v[24:27]\n"
      "ds_read_b64_tr_b16 v[102:103], v3 offset:29184\n"
      "v_mfma_f32_16x16x32_bf16 v[32:35], v[136:139], v[120:123], v[32:35]\n"
      "ds_read_b128 v[72:75], v1 offset:20480\n"
      "v_mfma_f32_16x16x32_bf16 v[24:27], v[144:147], v[120:123], v[24:27]\n"
      "ds_read_b64_tr_b16 v[92:93], v3 offset:24576\n"
      "v_mfma_f32_16x16x32_bf16 v[16:19], v[152:155], v[120:123], v[16:19]\n"
      "ds_read_b64_tr_b16 v[94:95], v3 offset:25088\n"
      "v_mfma_f32_16x16x32_bf16 v[40:43], v[136:139], v[124:127], v[40:43]\n"
      "ds_read_b128 v[76:79], v1 offset:22528\n"
      "v_mfma_f32_16x16x32_bf16 v[32:35], v[144:147], v[124:127], v[32:35]\n"
      "ds_read_b64_tr_b16 v[108:109], v3 offset:32768\n"
      "v_mfma_f32_16x16x32_bf16 v[24:27], v[152:155], v[124:127], v[24:27]\n"
      "ds_read_b64_tr_b16 v[110:111], v3 offset:33280\n"
      "v_mfma_f32_16x16x32_bf16 v[40:43], v[144:147], v[128:131], v[40:43]\n"
      "ds_read_b128 v[80:83], v1 offset:24576\n"
      "v_mfma_f32_16x16x32_bf16 v[32:35], v[152:155], v[128:131], v[32:35]\n"
      "ds_read_b128 v[84:87], v1 offset:26624\n"
      "v_mfma_f32_16x16x32_bf16 v[48:51], v[136:139], v[132:135], v[48:51]\n"
      "ds_read_b128 v[88:91], v1 offset:28672\n"
      "v_mfma_f32_16x16x32_bf16 v[56:59], v[144:147], v[132:135], v[56:59]\n"
      "ds_read_b64_tr_b16 v[104:105], v4 offset:28672\n"
      "v_mfma_f32_16x16x32_bf16 v[64:67], v[152:155], v[132:135], v[64:67]\n"
      "ds_read_b64_tr_b16 v[106:107], v4 offset:29184\n"
      "v_mfma_f32_16x16x32_bf16 v[20:23], v[148:151], v[116:119], v[20:23]\n"
      "ds_read_b64_tr_b16 v[96:97], v4 offset:24576\n"
      "v_mfma_f32_16x16x32_bf16 v[28:31], v[140:143], v[116:119], v[28:31]\n"
      "ds_read_b64_tr_b16 v[98:99], v4 offset:25088\n"
      "v_mfma_f32_16x16x32_bf16 v[36:39], v[140:143], v[120:123], v[36:39]\n"
      "ds_read_b64_tr_b16 v[112:113], v4 offset:32768\n"
      "v_mfma_f32_16x16x32_bf16 v[28:31], v[148:151], v[120:123], v[28:31]\n"
      "ds_read_b64_tr_b16 v[114:115], v4 offset:33280\n"
      "v_mfma_f32_16x16x32_bf16 v[20:23], v[156:159], v[120:123], v[20:23]\n"
      "v_mfma_f32_16x16x32_bf16 v[44:47], v[140:143], v[124:127], v[44:47]\n"
      "v_mfma_f32_16x16x32_bf16 v[36:39], v[148:151], v[124:127], v[36:39]\n"
      "v_mfma_f32_16x16x32_bf16 v[28:31], v[156:159], v[124:127], v[28:31]\n"
      "v_mfma_f32_16x16x32_bf16 v[44:47], v[148:151], v[128:131], v[44:47]\n"
      "v_mfma_f32_16x16x32_bf16 v[36:39], v[156:159], v[128:131], v[36:39]\n"
      "v_mfma_f32_16x16x32_bf16 v[52:55], v[140:143], v[132:135], v[52:55]\n"
      "v_mfma_f32_16x16x32_bf16 v[60:63], v[148:151], v[132:135], v[60:63]\n"
      "v_mfma_f32_16x16x32_bf16 v[68:71], v[156:159], v[132:135], v[68:71]\n"
      "s_waitcnt lgkmcnt(0)\n"
      "s_barrier\n"
      "v_mfma_f32_16x16x32_bf16 v[16:19], v[100:103], v[72:75], v[16:19]\n"
      "ds_read_b64_tr_b16 v[144:145], v5 offset:4096\n"
      "v_mfma_f32_16x16x32_bf16 v[24:27], v[92:95], v[72:75], v[24:27]\n"
      "ds_read_b64_tr_b16 v[146:147], v5 offset:4608\n"
      "v_mfma_f32_16x16x32_bf16 v[32:35], v[92:95], v[76:79], v[32:35]\n"
      "ds_read_b128 v[116:119], v2 offset:20480\n"
      "v_mfma_f32_16x16x32_bf16 v[24:27], v[100:103], v[76:79], v[24:27]\n"
      "ds_read_b64_tr_b16 v[136:137], v5 offset:0\n"
      "v_mfma_f32_16x16x32_bf16 v[16:19], v[108:111], v[76:79], v[16:19]\n"
      "ds_read_b64_tr_b16 v[138:139], v5 offset:512\n"
      "v_mfma_f32_16x16x32_bf16 v[40:43], v[92:95], v[80:83], v[40:43]\n"
      "ds_read_b128 v[120:123], v2 offset:22528\n"
      "v_mfma_f32_16x16x32_bf16 v[32:35], v[100:103], v[80:83], v[32:35]\n"
      "ds_read_b64_tr_b16 v[152:153], v5 offset:8192\n"
      "v_mfma_f32_16x16x32_bf16 v[24:27], v[108:111], v[80:83], v[24:27]\n"
      "ds_read_b64_tr_b16 v[154:155], v5 offset:8704\n"
      "v_mfma_f32_16x16x32_bf16 v[40:43], v[100:103], v[84:87], v[40:43]\n"
      "ds_read_b128 v[124:127], v2 offset:24576\n"
      "v_mfma_f32_16x16x32_bf16 v[32:35], v[108:111], v[84:87], v[32:35]\n"
      "ds_read_b128 v[128:131], v2 offset:26624\n"
      "v_mfma_f32_16x16x32_bf16 v[48:51], v[92:95], v[88:91], v[48:51]\n"
      "ds_read_b128 v[132:135], v2 offset:28672\n"
      "v_mfma_f32_16x16x32_bf16 v[56:59], v[100:103], v[88:91], v[56:59]\n"
      "ds_read_b64_tr_b16 v[148:149], v6 offset:4096\n"
      "v_mfma_f32_16x16x32_bf16 v[64:67], v[108:111], v[88:91], v[64:67]\n"
      "ds_read_b64_tr_b16 v[150:151], v6 offset:4608\n"
      "v_mfma_f32_16x16x32_bf16 v[20:23], v[104:107], v[72:75], v[20:23]\n"
      "ds_read_b64_tr_b16 v[140:141], v6 offset:0\n"
      "v_mfma_f32_16x16x32_bf16 v[28:31], v[96:99], v[72:75], v[28:31]\n"
      "ds_read_b64_tr_b16 v[142:143], v6 offset:512\n"
      "v_mfma_f32_16x16x32_bf16 v[36:39], v[96:99], v[76:79], v[36:39]\n"
      "ds_read_b64_tr_b16 v[156:157], v6 offset:8192\n"
      "v_mfma_f32_16x16x32_bf16 v[28:31], v[104:107], v[76:79], v[28:31]\n"
      "ds_read_b64_tr_b16 v[158:159], v6 offset:8704\n"
      "v_mfma_f32_16x16x32_bf16 v[20:23], v[112:115], v[76:79], v[20:23]\n"
      "v_mfma_f32_16x16x32_bf16 v[44:47], v[96:99], v[80:83], v[44:47]\n"
      "v_mfma_f32_16x16x32_bf16 v[36:39], v[104:107], v[80:83], v[36:39]\n"
      "v_mfma_f32_16x16x32_bf16 v[28:31], v[112:115], v[80:83], v[28:31]\n"
      "v_mfma_f32_16x16x32_bf16 v[44:47], v[104:107], v[84:87], v[44:47]\n"
      "v_mfma_f32_16x16x32_bf16 v[36:39], v[112:115], v[84:87], v[36:39]\n"
      "v_mfma_f32_16x16x32_bf16 v[52:55], v[96:99], v[88:91], v[52:55]\n"
      "v_mfma_f32_16x16x32_bf16 v[60:63], v[104:107], v[88:91], v[60:63]\n"
      "v_mfma_f32_16x16x32_bf16 v[68:71], v[112:115], v[88:91], v[68:71]\n"
      "s_cmp_eq_u32 s35, 2\n"
      "s_cbranch_scc1 Ltail%=\n"
      "s_waitcnt lgkmcnt(0)\n"
      "s_barrier\n"
      "v_mfma_f32_16x16x32_bf16 v[16:19], v[144:147], v[116:119], v[16:19]\n"
      "ds_read_b64_tr_b16 v[100:101], v5 offset:16384\n"
      "v_mfma_f32_16x16x32_bf16 v[24:27], v[136:139], v[116:119], v[24:27]\n"
      "ds_read_b64_tr_b16 v[102:103], v5 offset:16896\n"
      "v_mfma_f32_16x16x32_bf16 v[32:35], v[136:139], v[120:123], v[32:35]\n"
      "ds_read_b128 v[72:75], v1 offset:40960\n"
      "v_mfma_f32_16x16x32_bf16 v[24:27], v[144:147], v[120:123], v[24:27]\n"
      "ds_read_b64_tr_b16 v[92:93], v5 offset:12288\n"
      "v_mfma_f32_16x16x32_bf16 v[16:19], v[152:155], v[120:123], v[16:19]\n"
      "ds_read_b64_tr_b16 v[94:95], v5 offset:12800\n"
      "v_mfma_f32_16x16x32_bf16 v[40:43], v[136:139], v[124:127], v[40:43]\n"
      "ds_read_b128 v[76:79], v1 offset:43008\n"
      "v_mfma_f32_16x16x32_bf16 v[32:35], v[144:147], v[124:127], v[32:35]\n"
      "ds_read_b64_tr_b16 v[108:109], v5 offset:20480\n"
      "v_mfma_f32_16x16x32_bf16 v[24:27], v[152:155], v[124:127], v[24:27]\n"
      "ds_read_b64_tr_b16 v[110:111], v5 offset:20992\n"
      "v_mfma_f32_16x16x32_bf16 v[40:43], v[144:147], v[128:131], v[40:43]\n"
      "ds_read_b128 v[80:83], v1 offset:45056\n"
      "v_mfma_f32_16x16x32_bf16 v[32:35], v[152:155], v[128:131], v[32:35]\n"
      "ds_read_b128 v[84:87], v1 offset:47104\n"
      "v_mfma_f32_16x16x32_bf16 v[48:51], v[136:139], v[132:135], v[48:51]\n"
      "ds_read_b128 v[88:91], v1 offset:49152\n"
      "v_mfma_f32_16x16x32_bf16 v[56:59], v[144:147], v[132:135], v[56:59]\n"
      "ds_read_b64_tr_b16 v[104:105], v6 offset:16384\n"
      "v_mfma_f32_16x16x32_bf16 v[64:67], v[152:155], v[132:135], v[64:67]\n"
      "ds_read_b64_tr_b16 v[106:107], v6 offset:16896\n"
      "v_mfma_f32_16x16x32_bf16 v[20:23], v[148:151], v[116:119], v[20:23]\n"
      "ds_read_b64_tr_b16 v[96:97], v6 offset:12288\n"
      "v_mfma_f32_16x16x32_bf16 v[28:31], v[140:143], v[116:119], v[28:31]\n"
      "ds_read_b64_tr_b16 v[98:99], v6 offset:12800\n"
      "v_mfma_f32_16x16x32_bf16 v[36:39], v[140:143], v[120:123], v[36:39]\n"
      "ds_read_b64_tr_b16 v[112:113], v6 offset:20480\n"
      "v_mfma_f32_16x16x32_bf16 v[28:31], v[148:151], v[120:123], v[28:31]\n"
      "ds_read_b64_tr_b16 v[114:115], v6 offset:20992\n"
      "v_mfma_f32_16x16x32_bf16 v[20:23], v[156:159], v[120:123], v[20:23]\n"
      "v_mfma_f32_16x16x32_bf16 v[44:47], v[140:143], v[124:127], v[44:47]\n"
      "v_mfma_f32_16x16x32_bf16 v[36:39], v[148:151], v[124:127], v[36:39]\n"
      "v_mfma_f32_16x16x32_bf16 v[28:31], v[156:159], v[124:127], v[28:31]\n"
      "v_mfma_f32_16x16x32_bf16 v[44:47], v[148:151], v[128:131], v[44:47]\n"
      "v_mfma_f32_16x16x32_bf16 v[36:39], v[156:159], v[128:131], v[36:39]\n"
      "v_mfma_f32_16x16x32_bf16 v[52:55], v[140:143], v[132:135], v[52:55]\n"
      "v_mfma_f32_16x16x32_bf16 v[60:63], v[148:151], v[132:135], v[60:63]\n"
      "v_mfma_f32_16x16x32_bf16 v[68:71], v[156:159], v[132:135], v[68:71]\n"
      "s_waitcnt lgkmcnt(0)\n"
      "s_barrier\n"
      "v_mfma_f32_16x16x32_bf16 v[16:19], v[100:103], v[72:75], v[16:19]\n"
      "ds_read_b64_tr_b16 v[144:145], v5 offset:28672\n"
      "v_mfma_f32_16x16x32_bf16 v[24:27], v[92:95], v[72:75], v[24:27]\n"
      "ds_read_b64_tr_b16 v[146:147], v5 offset:29184\n"
      "v_mfma_f32_16x16x32_bf16 v[32:35], v[92:95], v[76:79], v[32:35]\n"
      "ds_read_b128 v[116:119], v2 offset:40960\n"
      "v_mfma_f32_16x16x32_bf16 v[24:27], v[100:103], v[76:79], v[24:27]\n"
      "ds_read_b64_tr_b16 v[136:137], v5 offset:24576\n"
      "v_mfma_f32_16x16x32_bf16 v[16:19], v[108:111], v[76:79], v[16:19]\n"
      "ds_read_b64_tr_b16 v[138:139], v5 offset:25088\n"
      "v_mfma_f32_16x16x32_bf16 v[40:43], v[92:95], v[80:83], v[40:43]\n"
      "ds_read_b128 v[120:123], v2 offset:43008\n"
      "v_mfma_f32_16x16x32_bf16 v[32:35], v[100:103], v[80:83], v[32:35]\n"
      "ds_read_b64_tr_b16 v[152:153], v5 offset:32768\n"
      "v_mfma_f32_16x16x32_bf16 v[24:27], v[108:111], v[80:83], v[24:27]\n"
      "ds_read_b64_tr_b16 v[154:155], v5 offset:33280\n"
      "v_mfma_f32_16x16x32_bf16 v[40:43], v[100:103], v[84:87], v[40:43]\n"
      "ds_read_b128 v[124:127], v2 offset:45056\n"
      "v_mfma_f32_16x16x32_bf16 v[32:35], v[108:111], v[84:87], v[32:35]\n"
      "ds_read_b128 v[128:131], v2 offset:47104\n"
      "v_mfma_f32_16x16x32_bf16 v[48:51], v[92:95], v[88:91], v[48:51]\n"
      "ds_read_b128 v[132:135], v2 offset:49152\n"
      "v_mfma_f32_16x16x32_bf16 v[56:59], v[100:103], v[88:91], v[56:59]\n"
      "ds_read_b64_tr_b16 v[148:149], v6 offset:28672\n"
      "v_mfma_f32_16x16x32_bf16 v[64:67], v[108:111], v[88:91], v[64:67]\n"
      "ds_read_b64_tr_b16 v[150:151], v6 offset:29184\n"
      "v_mfma_f32_16x16x32_bf16 v[20:23], v[104:107], v[72:75], v[20:23]\n"
      "ds_read_b64_tr_b16 v[140:141], v6 offset:24576\n"
      "v_mfma_f32_16x16x32_bf16 v[28:31], v[96:99], v[72:75], v[28:31]\n"
      "ds_read_b64_tr_b16 v[142:143], v6 offset:25088\n"
      "v_mfma_f32_16x16x32_bf16 v[36:39], v[96:99], v[76:79], v[36:39]\n"
      "ds_read_b64_tr_b16 v[156:157], v6 offset:32768\n"
      "v_mfma_f32_16x16x32_bf16 v[28:31], v[104:107], v[76:79], v[28:31]\n"
      "ds_read_b64_tr_b16 v[158:159], v6 offset:33280\n"
      "v_mfma_f32_16x16x32_bf16 v[20:23], v[112:115], v[76:79], v[20:23]\n"
      "v_mfma_f32_16x16x32_bf16 v[44:47], v[96:99], v[80:83], v[44:47]\n"
      "v_mfma_f32_16x16x32_bf16 v[36:39], v[104:107], v[80:83], v[36:39]\n"
      "v_mfma_f32_16x16x32_bf16 v[28:31], v[112:115], v[80:83], v[28:31]\n"
      "v_mfma_f32_16x16x32_bf16 v[44:47], v[104:107], v[84:87], v[44:47]\n"
      "v_mfma_f32_16x16x32_bf16 v[36:39], v[112:115], v[84:87], v[36:39]\n"
      "v_mfma_f32_16x16x32_bf16 v[52:55], v[96:99], v[88:91], v[52:55]\n"
      "v_mfma_f32_16x16x32_bf16 v[60:63], v[104:107], v[88:91], v[60:63]\n"
      "v_mfma_f32_16x16x32_bf16 v[68:71], v[112:115], v[88:91], v[68:71]\n"
      "s_waitcnt lgkmcnt(0)\n"
      "s_barrier\n"
      "v_mfma_f32_16x16x32_bf16 v[16:19], v[144:147], v[116:119], v[16:19]\n"
      "ds_read_b64_tr_b16 v[100:101], v3 offset:4096\n"
      "v_mfma_f32_16x16x32_bf16 v[24:27], v[136:139], v[116:119], v[24:27]\n"
      "ds_read_b64_tr_b16 v[102:103], v3 offset:4608\n"
      "v_mfma_f32_16x16x32_bf16 v[32:35], v[136:139], v[120:123], v[32:35]\n"
      "ds_read_b128 v[72:75], v1 offset:0\n"
      "v_mfma_f32_16x16x32_bf16 v[24:27], v[144:147], v[120:123], v[24:27]\n"
      "ds_read_b64_tr_b16 v[92:93], v3 offset:0\n"
      "v_mfma_f32_16x16x32_bf16 v[16:19], v[152:155], v[120:123], v[16:19]\n"
      "ds_read_b64_tr_b16 v[94:95], v3 offset:512\n"
      "v_mfma_f32_16x16x32_bf16 v[40:43], v[136:139], v[124:127], v[40:43]\n"
      "ds_read_b128 v[76:79], v1 offset:2048\n"
      "v_mfma_f32_16x16x32_bf16 v[32:35], v[144:147], v[124:127], v[32:35]\n"
      "ds_read_b64_tr_b16 v[108:109], v3 offset:8192\n"
      "v_mfma_f32_16x16x32_bf16 v[24:27], v[152:155], v[124:127], v[24:27]\n"
      "ds_read_b64_tr_b16 v[110:111], v3 offset:8704\n"
      "v_mfma_f32_16x16x32_bf16 v[40:43], v[144:147], v[128:131], v[40:43]\n"
      "ds_read_b128 v[80:83], v1 offset:4096\n"
      "v_mfma_f32_16x16x32_bf16 v[32:35], v[152:155], v[128:131], v[32:35]\n"
      "ds_read_b128 v[84:87], v1 offset:6144\n"
      "v_mfma_f32_16x16x32_bf16 v[48:51], v[136:139], v[132:135], v[48:51]\n"
      "ds_read_b128 v[88:91], v1 offset:8192\n"
      "v_mfma_f32_16x16x32_bf16 v[56:59], v[144:147], v[132:135], v[56:59]\n"
      "ds_read_b64_tr_b16 v[104:105], v4 offset:4096\n"
      "v_mfma_f32_16x16x32_bf16 v[64:67], v[152:155], v[132:135], v[64:67]\n"
      "ds_read_b64_tr_b16 v[106:107], v4 offset:4608\n"
      "v_mfma_f32_16x16x32_bf16 v[20:23], v[148:151], v[116:119], v[20:23]\n"
      "ds_read_b64_tr_b16 v[96:97], v4 offset:0\n"
      "v_mfma_f32_16x16x32_bf16 v[28:31], v[140:143], v[116:119], v[28:31]\n"
      "ds_read_b64_tr_b16 v[98:99], v4 offset:512\n"
      "v_mfma_f32_16x16x32_bf16 v[36:39], v[140:143], v[120:123], v[36:39]\n"
      "ds_read_b64_tr_b16 v[112:113], v4 offset:8192\n"
      "v_mfma_f32_16x16x32_bf16 v[28:31], v[148:151], v[120:123], v[28:31]\n"
      "ds_read_b64_tr_b16 v[114:115], v4 offset:8704\n"
      "v_mfma_f32_16x16x32_bf16 v[20:23], v[156:159], v[120:123], v[20:23]\n"
      "v_mfma_f32_16x16x32_bf16 v[44:47], v[140:143], v[124:127], v[44:47]\n"
      "v_mfma_f32_16x16x32_bf16 v[36:39], v[148:151], v[124:127], v[36:39]\n"
      "v_mfma_f32_16x16x32_bf16 v[28:31], v[156:159], v[124:127], v[28:31]\n"
      "v_mfma_f32_16x16x32_bf16 v[44:47], v[148:151], v[128:131], v[44:47]\n"
      "v_mfma_f32_16x16x32_bf16 v[36:39], v[156:159], v[128:131], v[36:39]\n"
      "v_mfma_f32_16x16x32_bf16 v[52:55], v[140:143], v[132:135], v[52:55]\n"
      "v_mfma_f32_16x16x32_bf16 v[60:63], v[148:151], v[132:135], v[60:63]\n"
      "v_mfma_f32_16x16x32_bf16 v[68:71], v[156:159], v[132:135], v[68:71]\n"
      "s_add_u32 s35, s35, 1\n"
      "s_branch Lloop%=\n"
      "Ltail%=:\n"
      "s_waitcnt lgkmcnt(0)\n"
      "s_barrier\n"
      "v_mfma_f32_16x16x32_bf16 v[16:19], v[144:147], v[116:119], v[16:19]\n"
      "ds_read_b64_tr_b16 v[100:101], v5 offset:16384\n"
      "ds_read_b64_tr_b16 v[102:103], v5 offset:16896\n"
      "v_mfma_f32_16x16x32_bf16 v[20:23], v[148:151], v[116:119], v[20:23]\n"
      "ds_read_b128 v[72:75], v1 offset:40960\n"
      "ds_read_b64_tr_b16 v[104:105], v6 offset:16384\n"
      "v_mfma_f32_16x16x32_bf16 v[16:19], v[152:155], v[120:123], v[16:19]\n"
      "ds_read_b64_tr_b16 v[106:107], v6 offset:16896\n"
      "ds_read_b64_tr_b16 v[108:109], v5 offset:20480\n"
      "v_mfma_f32_16x16x32_bf16 v[20:23], v[156:159], v[120:123], v[20:23]\n"
      "ds_read_b64_tr_b16 v[110:111], v5 offset:20992\n"
      "ds_read_b128 v[76:79], v1 offset:43008\n"
      "v_mfma_f32_16x16x32_bf16 v[40:43], v[136:139], v[124:127], v[40:43]\n"
      "ds_read_b64_tr_b16 v[112:113], v6 offset:20480\n"
      "ds_read_b64_tr_b16 v[114:115], v6 offset:20992\n"
      "v_mfma_f32_16x16x32_bf16 v[44:47], v[140:143], v[124:127], v[44:47]\n"
      "ds_read_b64_tr_b16 v[228:229], v5 offset:28672\n"
      "ds_read_b64_tr_b16 v[230:231], v5 offset:29184\n"
      "v_mfma_f32_16x16x32_bf16 v[40:43], v[144:147], v[128:131], v[40:43]\n"
      "ds_read_b128 v[200:203], v2 offset:40960\n"
      "ds_read_b64_tr_b16 v[232:233], v6 offset:28672\n"
      "v_mfma_f32_16x16x32_bf16 v[44:47], v[148:151], v[128:131], v[44:47]\n"
      "ds_read_b64_tr_b16 v[234:235], v6 offset:29184\n"
      "ds_read_b64_tr_b16 v[236:237], v5 offset:32768\n"
      "v_mfma_f32_16x16x32_bf16 v[48:51], v[136:139], v[132:135], v[48:51]\n"
      "ds_read_b64_tr_b16 v[238:239], v5 offset:33280\n"
      "ds_read_b128 v[204:207], v2 offset:43008\n"
      "v_mfma_f32_16x16x32_bf16 v[52:55], v[140:143], v[132:135], v[52:55]\n"
      "ds_read_b64_tr_b16 v[240:241], v6 offset:32768\n"
      "ds_read_b64_tr_b16 v[242:243], v6 offset:33280\n"
      "v_mfma_f32_16x16x32_bf16 v[56:59], v[144:147], v[132:135], v[56:59]\n"
      "ds_read_b128 v[160:163], v7 offset:0\n"
      "ds_read_b128 v[164:167], v7 offset:64\n"
      "v_mfma_f32_16x16x32_bf16 v[60:63], v[148:151], v[132:135], v[60:63]\n"
      "ds_read_b128 v[168:171], v7 offset:256\n"
      "ds_read_b128 v[172:175], v7 offset:320\n"
      "v_mfma_f32_16x16x32_bf16 v[64:67], v[152:155], v[132:135], v[64:67]\n"
      "ds_read_b64_tr_b16 v[92:93], v5 offset:12288\n"
      "ds_read_b64_tr_b16 v[94:95], v5 offset:12800\n"
      "v_mfma_f32_16x16x32_bf16 v[68:71], v[156:159], v[132:135], v[68:71]\n"
      "ds_read_b128 v[80:83], v1 offset:45056\n"
      "ds_read_b64_tr_b16 v[96:97], v6 offset:12288\n"
      "s_waitcnt lgkmcnt(15)\n"
      "v_mfma_f32_16x16x32_bf16 v[16:19], v[100:103], v[72:75], v[16:19]\n"
      "ds_read_b64_tr_b16 v[98:99], v6 offset:12800\n"
      "v_mfma_f32_16x16x32_bf16 v[20:23], v[104:107], v[72:75], v[20:23]\n"
      "ds_read_b128 v[84:87], v1 offset:47104\n"
      "v_mfma_f32_16x16x32_bf16 v[16:19], v[108:111], v[76:79], v[16:19]\n"
      "ds_read_b64_tr_b16 v[220:221], v5 offset:24576\n"
      "v_mfma_f32_16x16x32_bf16 v[20:23], v[112:115], v[76:79], v[20:23]\n"
      "ds_read_b64_tr_b16 v[222:223], v5 offset:25088\n"
      "v_mfma_f32_16x16x32_bf16 v[16:19], v[228:231], v[200:203], v[16:19]\n"
      "ds_read_b128 v[208:211], v2 offset:45056\n"
      "s_waitcnt lgkmcnt(15)\n"
      "v_mfma_f32_16x16x32_bf16 v[20:23], v[232:235], v[200:203], v[20:23]\n"
      "ds_read_b64_tr_b16 v[224:225], v6 offset:24576\n"
      "v_mfma_f32_16x16x32_bf16 v[16:19], v[236:239], v[204:207], v[16:19]\n"
      "ds_read_b64_tr_b16 v[226:227], v6 offset:25088\n"
      "s_waitcnt lgkmcnt(15)\n"
      "v_mfma_f32_16x16x32_bf16 v[20:23], v[240:243], v[204:207], v[20:23]\n"
      "ds_read_b128 v[212:215], v2 offset:47104\n"
      "s_waitcnt lgkmcnt(9)\n"
      "v_mfma_f32_16x16x32_bf16 v[40:43], v[92:95], v[80:83], v[40:43]\n"
      "ds_read_b128 v[88:91], v1 offset:49152\n"
      "s_nop 2\n"
      "s_waitcnt lgkmcnt(8)\n"
      "v_mfma_f32_16x16x32_bf16 v[44:47], v[96:99], v[80:83], v[44:47]\n"
      "ds_read_b128 v[216:219], v2 offset:49152\n"
      "s_nop 3\n"
      "v_pk_fma_f32 v[16:17], v[16:17], v[160:161], v[168:169]\n"
      "v_pk_fma_f32 v[18:19], v[18:19], v[162:163], v[170:171]\n"
      "v_pk_fma_f32 v[20:21], v[20:21], v[164:165], v[172:173]\n"
      "v_pk_fma_f32 v[22:23], v[22:23], v[166:167], v[174:175]\n"
      "s_waitcnt lgkmcnt(8)\n"
      "v_mfma_f32_16x16x32_bf16 v[40:43], v[100:103], v[84:87], v[40:43]\n"
      "v_exp_f32 v176, v16\n"
      "v_mfma_f32_16x16x32_bf16 v[44:47], v[104:107], v[84:87], v[44:47]\n"
      "v_exp_f32 v177, v17\n"
      "s_waitcnt lgkmcnt(5)\n"
      "v_mfma_f32_16x16x32_bf16 v[40:43], v[220:223], v[208:211], v[40:43]\n"
      "v_exp_f32 v178, v18\n"
      "s_waitcnt lgkmcnt(3)\n"
      "v_mfma_f32_16x16x32_bf16 v[44:47], v[224:227], v[208:211], v[44:47]\n"
      "v_exp_f32 v179, v19\n"
      "s_waitcnt lgkmcnt(2)\n"
      "v_mfma_f32_16x16x32_bf16 v[40:43], v[228:231], v[212:215], v[40:43]\n"
      "v_exp_f32 v180, v20\n"
      "v_mfma_f32_16x16x32_bf16 v[44:47], v[232:235], v[212:215], v[44:47]\n"
      "v_exp_f32 v181, v21\n"
      "s_waitcnt lgkmcnt(1)\n"
      "v_mfma_f32_16x16x32_bf16 v[48:51], v[92:95], v[88:91], v[48:51]\n"
      "v_exp_f32 v182, v22\n"
      "v_exp_f32 v183, v23\n"
      "v_pk_fma_f32 v[176:177], v[176:177], s[36:37], s[36:37]\n"
      "v_pk_fma_f32 v[178:179], v[178:179], s[36:37], s[36:37]\n"
      "v_pk_fma_f32 v[180:181], v[180:181], s[36:37], s[36:37]\n"
      "v_pk_fma_f32 v[182:183], v[182:183], s[36:37], s[36:37]\n"
      "v_mfma_f32_16x16x32_bf16 v[52:55], v[96:99], v[88:91], v[52:55]\n"
      "v_rcp_f32 v176, v176\n"
      "v_mfma_f32_16x16x32_bf16 v[56:59], v[100:103], v[88:91], v[56:59]\n"
      "v_rcp_f32 v177, v177\n"
      "v_mfma_f32_16x16x32_bf16 v[60:63], v[104:107], v[88:91], v[60:63]\n"
      "v_rcp_f32 v178, v178\n"
      "v_mfma_f32_16x16x32_bf16 v[64:67], v[108:111], v[88:91], v[64:67]\n"
      "v_rcp_f32 v179, v179\n"
      "v_mfma_f32_16x16x32_bf16 v[68:71], v[112:115], v[88:91], v[68:71]\n"
      "v_rcp_f32 v180, v180\n"
      "s_waitcnt lgkmcnt(0)\n"
      "v_mfma_f32_16x16x32_bf16 v[48:51], v[220:223], v[216:219], v[48:51]\n"
      "v_rcp_f32 v181, v181\n"
      "v_mfma_f32_16x16x32_bf16 v[52:55], v[224:227], v[216:219], v[52:55]\n"
      "v_rcp_f32 v182, v182\n"
      "v_rcp_f32 v183, v183\n"
      "v_pk_mul_f32 v[16:17], v[16:17], v[176:177]\n"
      "v_pk_mul_f32 v[18:19], v[18:19], v[178:179]\n"
      "v_pk_mul_f32 v[20:21], v[20:21], v[180:181]\n"
      "v_pk_mul_f32 v[22:23], v[22:23], v[182:183]\n"
      "v_cvt_pk_bf16_f32 v176, v16, v17\n"
      "v_cvt_pk_bf16_f32 v178, v20, v21\n"
      "v_cvt_pk_bf16_f32 v177, v18, v19\n"
      "v_cvt_pk_bf16_f32 v179, v22, v23\n"
      "v_permlane16_swap_b32 v176, v178\n"
      "s_nop 0\n"
      "v_permlane16_swap_b32 v177, v179\n"
      "buffer_store_dwordx4 v[176:179], v15, s[12:15], 0 offen offset:0 sc0 sc1\n"
      "v_pk_fma_f32 v[40:41], v[40:41], v[160:161], v[168:169]\n"
      "v_pk_fma_f32 v[42:43], v[42:43], v[162:163], v[170:171]\n"
      "v_pk_fma_f32 v[44:45], v[44:45], v[164:165], v[172:173]\n"
      "v_pk_fma_f32 v[46:47], v[46:47], v[166:167], v[174:175]\n"
      "v_mfma_f32_16x16x32_bf16 v[56:59], v[228:231], v[216:219], v[56:59]\n"
      "v_exp_f32 v184, v40\n"
      "v_mfma_f32_16x16x32_bf16 v[60:63], v[232:235], v[216:219], v[60:63]\n"
      "v_exp_f32 v185, v41\n"
      "v_mfma_f32_16x16x32_bf16 v[64:67], v[236:239], v[216:219], v[64:67]\n"
      "v_exp_f32 v186, v42\n"
      "v_mfma_f32_16x16x32_bf16 v[68:71], v[240:243], v[216:219], v[68:71]\n"
      "v_exp_f32 v187, v43\n"
      "v_mfma_f32_16x16x32_bf16 v[24:27], v[136:139], v[116:119], v[24:27]\n"
      "v_exp_f32 v188, v44\n"
      "v_mfma_f32_16x16x32_bf16 v[28:31], v[140:143], v[116:119], v[28:31]\n"
      "v_exp_f32 v189, v45\n"
      "v_mfma_f32_16x16x32_bf16 v[24:27], v[144:147], v[120:123], v[24:27]\n"
      "v_exp_f32 v190, v46\n"
      "v_exp_f32 v191, v47\n"
      "v_pk_fma_f32 v[184:185], v[184:185], s[36:37], s[36:37]\n"
      "v_pk_fma_f32 v[186:187], v[186:187], s[36:37], s[36:37]\n"
      "v_pk_fma_f32 v[188:189], v[188:189], s[36:37], s[36:37]\n"
      "v_pk_fma_f32 v[190:191], v[190:191], s[36:37], s[36:37]\n"
      "v_mfma_f32_16x16x32_bf16 v[28:31], v[148:151], v[120:123], v[28:31]\n"
      "v_rcp_f32 v184, v184\n"
      "v_mfma_f32_16x16x32_bf16 v[24:27], v[152:155], v[124:127], v[24:27]\n"
      "v_rcp_f32 v185, v185\n"
      "v_mfma_f32_16x16x32_bf16 v[28:31], v[156:159], v[124:127], v[28:31]\n"
      "v_rcp_f32 v186, v186\n"
      "v_mfma_f32_16x16x32_bf16 v[24:27], v[92:95], v[72:75], v[24:27]\n"
      "v_rcp_f32 v187, v187\n"
      "v_mfma_f32_16x16x32_bf16 v[28:31], v[96:99], v[72:75], v[28:31]\n"
      "v_rcp_f32 v188, v188\n"
      "v_mfma_f32_16x16x32_bf16 v[24:27], v[100:103], v[76:79], v[24:27]\n"
      "v_rcp_f32 v189, v189\n"
      "v_mfma_f32_16x16x32_bf16 v[28:31], v[104:107], v[76:79], v[28:31]\n"
      "v_rcp_f32 v190, v190\n"
      "v_rcp_f32 v191, v191\n"
      "v_pk_mul_f32 v[40:41], v[40:41], v[184:185]\n"
      "v_pk_mul_f32 v[42:43], v[42:43], v[186:187]\n"
      "v_pk_mul_f32 v[44:45], v[44:45], v[188:189]\n"
      "v_pk_mul_f32 v[46:47], v[46:47], v[190:191]\n"
      "v_cvt_pk_bf16_f32 v184, v40, v41\n"
      "v_cvt_pk_bf16_f32 v186, v44, v45\n"
      "v_cvt_pk_bf16_f32 v185, v42, v43\n"
      "v_cvt_pk_bf16_f32 v187, v46, v47\n"
      "v_permlane16_swap_b32 v184, v186\n"
      "s_nop 0\n"
      "v_permlane16_swap_b32 v185, v187\n"
      "buffer_store_dwordx4 v[184:187], v15, s[12:15], 0 offen offset:3456 sc0 sc1\n"
      "v_add_f32_dpp v56, v48, v56 row_shr:4 row_mask:0xf bank_mask:0xf bound_ctrl:1\n"
      "v_add_f32_dpp v57, v49, v57 row_shr:4 row_mask:0xf bank_mask:0xf bound_ctrl:1\n"
      "v_add_f32_dpp v58, v50, v58 row_shr:4 row_mask:0xf bank_mask:0xf bound_ctrl:1\n"
      "v_add_f32_dpp v59, v51, v59 row_shr:4 row_mask:0xf bank_mask:0xf bound_ctrl:1\n"
      "v_add_f32_dpp v60, v52, v60 row_shr:4 row_mask:0xf bank_mask:0xf bound_ctrl:1\n"
      "v_add_f32_dpp v61, v53, v61 row_shr:4 row_mask:0xf bank_mask:0xf bound_ctrl:1\n"
      "v_add_f32_dpp v62, v54, v62 row_shr:4 row_mask:0xf bank_mask:0xf bound_ctrl:1\n"
      "v_add_f32_dpp v63, v55, v63 row_shr:4 row_mask:0xf bank_mask:0xf bound_ctrl:1\n"
      "v_add_f32_dpp v56, v64, v56 row_shl:4 row_mask:0xf bank_mask:0xf bound_ctrl:1\n"
      "v_add_f32_dpp v57, v65, v57 row_shl:4 row_mask:0xf bank_mask:0xf bound_ctrl:1\n"
      "v_add_f32_dpp v58, v66, v58 row_shl:4 row_mask:0xf bank_mask:0xf bound_ctrl:1\n"
      "v_add_f32_dpp v59, v67, v59 row_shl:4 row_mask:0xf bank_mask:0xf bound_ctrl:1\n"
      "v_add_f32_dpp v60, v68, v60 row_shl:4 row_mask:0xf bank_mask:0xf bound_ctrl:1\n"
      "v_add_f32_dpp v61, v69, v61 row_shl:4 row_mask:0xf bank_mask:0xf bound_ctrl:1\n"
      "v_add_f32_dpp v62, v70, v62 row_shl:4 row_mask:0xf bank_mask:0xf bound_ctrl:1\n"
      "v_add_f32_dpp v63, v71, v63 row_shl:4 row_mask:0xf bank_mask:0xf bound_ctrl:1\n"
      "v_pk_fma_f32 v[56:57], v[56:57], v[160:161], v[168:169]\n"
      "v_pk_fma_f32 v[58:59], v[58:59], v[162:163], v[170:171]\n"
      "v_pk_fma_f32 v[60:61], v[60:61], v[164:165], v[172:173]\n"
      "v_pk_fma_f32 v[62:63], v[62:63], v[166:167], v[174:175]\n"
      "v_mfma_f32_16x16x32_bf16 v[24:27], v[108:111], v[80:83], v[24:27]\n"
      "v_exp_f32 v192, v56\n"
      "v_mfma_f32_16x16x32_bf16 v[28:31], v[112:115], v[80:83], v[28:31]\n"
      "v_exp_f32 v193, v57\n"
      "v_mfma_f32_16x16x32_bf16 v[24:27], v[220:223], v[200:203], v[24:27]\n"
      "v_exp_f32 v194, v58\n"
      "v_mfma_f32_16x16x32_bf16 v[28:31], v[224:227], v[200:203], v[28:31]\n"
      "v_exp_f32 v195, v59\n"
      "v_mfma_f32_16x16x32_bf16 v[24:27], v[228:231], v[204:207], v[24:27]\n"
      "v_exp_f32 v196, v60\n"
      "v_mfma_f32_16x16x32_bf16 v[28:31], v[232:235], v[204:207], v[28:31]\n"
      "v_exp_f32 v197, v61\n"
      "v_mfma_f32_16x16x32_bf16 v[24:27], v[236:239], v[208:211], v[24:27]\n"
      "v_exp_f32 v198, v62\n"
      "v_exp_f32 v199, v63\n"
      "v_pk_fma_f32 v[192:193], v[192:193], s[36:37], s[36:37]\n"
      "v_pk_fma_f32 v[194:195], v[194:195], s[36:37], s[36:37]\n"
      "v_pk_fma_f32 v[196:197], v[196:197], s[36:37], s[36:37]\n"
      "v_pk_fma_f32 v[198:199], v[198:199], s[36:37], s[36:37]\n"
      "v_mfma_f32_16x16x32_bf16 v[28:31], v[240:243], v[208:211], v[28:31]\n"
      "v_rcp_f32 v192, v192\n"
      "v_mfma_f32_16x16x32_bf16 v[32:35], v[136:139], v[120:123], v[32:35]\n"
      "v_rcp_f32 v193, v193\n"
      "v_mfma_f32_16x16x32_bf16 v[36:39], v[140:143], v[120:123], v[36:39]\n"
      "v_rcp_f32 v194, v194\n"
      "v_mfma_f32_16x16x32_bf16 v[32:35], v[144:147], v[124:127], v[32:35]\n"
      "v_rcp_f32 v195, v195\n"
      "v_mfma_f32_16x16x32_bf16 v[36:39], v[148:151], v[124:127], v[36:39]\n"
      "v_rcp_f32 v196, v196\n"
      "v_mfma_f32_16x16x32_bf16 v[32:35], v[152:155], v[128:131], v[32:35]\n"
      "v_rcp_f32 v197, v197\n"
      "v_mfma_f32_16x16x32_bf16 v[36:39], v[156:159], v[128:131], v[36:39]\n"
      "v_rcp_f32 v198, v198\n"
      "v_rcp_f32 v199, v199\n"
      "v_pk_mul_f32 v[56:57], v[56:57], v[192:193]\n"
      "v_pk_mul_f32 v[58:59], v[58:59], v[194:195]\n"
      "v_pk_mul_f32 v[60:61], v[60:61], v[196:197]\n"
      "v_pk_mul_f32 v[62:63], v[62:63], v[198:199]\n"
      "v_cvt_pk_bf16_f32 v192, v56, v57\n"
      "v_cvt_pk_bf16_f32 v194, v60, v61\n"
      "v_cvt_pk_bf16_f32 v193, v58, v59\n"
      "v_cvt_pk_bf16_f32 v195, v62, v63\n"
      "v_permlane16_swap_b32 v192, v194\n"
      "s_nop 0\n"
      "v_permlane16_swap_b32 v193, v195\n"
      "buffer_store_dwordx4 v[192:195], v14, s[12:15], 0 offen offset:0 sc0 sc1\n"
      "v_pk_fma_f32 v[24:25], v[24:25], v[160:161], v[168:169]\n"
      "v_pk_fma_f32 v[26:27], v[26:27], v[162:163], v[170:171]\n"
      "v_pk_fma_f32 v[28:29], v[28:29], v[164:165], v[172:173]\n"
      "v_pk_fma_f32 v[30:31], v[30:31], v[166:167], v[174:175]\n"
      "v_mfma_f32_16x16x32_bf16 v[32:35], v[92:95], v[76:79], v[32:35]\n"
      "v_exp_f32 v176, v24\n"
      "v_mfma_f32_16x16x32_bf16 v[36:39], v[96:99], v[76:79], v[36:39]\n"
      "v_exp_f32 v177, v25\n"
      "v_mfma_f32_16x16x32_bf16 v[32:35], v[100:103], v[80:83], v[32:35]\n"
      "v_exp_f32 v178, v26\n"
      "v_exp_f32 v179, v27\n"
      "v_mfma_f32_16x16x32_bf16 v[36:39], v[104:107], v[80:83], v[36:39]\n"
      "v_exp_f32 v180, v28\n"
      "v_mfma_f32_16x16x32_bf16 v[32:35], v[108:111], v[84:87], v[32:35]\n"
      "v_exp_f32 v181, v29\n"
      "v_mfma_f32_16x16x32_bf16 v[36:39], v[112:115], v[84:87], v[36:39]\n"
      "v_exp_f32 v182, v30\n"
      "v_exp_f32 v183, v31\n"
      "v_pk_fma_f32 v[176:177], v[176:177], s[36:37], s[36:37]\n"
      "v_pk_fma_f32 v[178:179], v[178:179], s[36:37], s[36:37]\n"
      "v_pk_fma_f32 v[180:181], v[180:181], s[36:37], s[36:37]\n"
      "v_pk_fma_f32 v[182:183], v[182:183], s[36:37], s[36:37]\n"
      "v_mfma_f32_16x16x32_bf16 v[32:35], v[220:223], v[204:207], v[32:35]\n"
      "v_rcp_f32 v176, v176\n"
      "v_mfma_f32_16x16x32_bf16 v[36:39], v[224:227], v[204:207], v[36:39]\n"
      "v_rcp_f32 v177, v177\n"
      "v_mfma_f32_16x16x32_bf16 v[32:35], v[228:231], v[208:211], v[32:35]\n"
      "v_rcp_f32 v178, v178\n"
      "v_rcp_f32 v179, v179\n"
      "v_mfma_f32_16x16x32_bf16 v[36:39], v[232:235], v[208:211], v[36:39]\n"
      "v_rcp_f32 v180, v180\n"
      "v_mfma_f32_16x16x32_bf16 v[32:35], v[236:239], v[212:215], v[32:35]\n"
      "v_rcp_f32 v181, v181\n"
      "v_mfma_f32_16x16x32_bf16 v[36:39], v[240:243], v[212:215], v[36:39]\n"
      "v_rcp_f32 v182, v182\n"
      "v_rcp_f32 v183, v183\n"
      "v_pk_mul_f32 v[24:25], v[24:25], v[176:177]\n"
      "v_pk_mul_f32 v[26:27], v[26:27], v[178:179]\n"
      "v_pk_mul_f32 v[28:29], v[28:29], v[180:181]\n"
      "v_pk_mul_f32 v[30:31], v[30:31], v[182:183]\n"
      "v_cvt_pk_bf16_f32 v176, v24, v25\n"
      "v_cvt_pk_bf16_f32 v178, v28, v29\n"
      "v_cvt_pk_bf16_f32 v177, v26, v27\n"
      "v_cvt_pk_bf16_f32 v179, v30, v31\n"
      "v_permlane16_swap_b32 v176, v178\n"
      "s_nop 0\n"
      "v_permlane16_swap_b32 v177, v179\n"
      "buffer_store_dwordx4 v[176:179], v15, s[12:15], 0 offen offset:1152 sc0 sc1\n"
      "s_nop 4\n"
      "v_pk_fma_f32 v[32:33], v[32:33], v[160:161], v[168:169]\n"
      "v_pk_fma_f32 v[34:35], v[34:35], v[162:163], v[170:171]\n"
      "v_pk_fma_f32 v[36:37], v[36:37], v[164:165], v[172:173]\n"
      "v_pk_fma_f32 v[38:39], v[38:39], v[166:167], v[174:175]\n"
      "v_exp_f32 v184, v32\n"
      "v_exp_f32 v185, v33\n"
      "v_exp_f32 v186, v34\n"
      "v_exp_f32 v187, v35\n"
      "v_exp_f32 v188, v36\n"
      "v_exp_f32 v189, v37\n"
      "v_exp_f32 v190, v38\n"
      "v_exp_f32 v191, v39\n"
      "v_pk_fma_f32 v[184:185], v[184:185], s[36:37], s[36:37]\n"
      "v_pk_fma_f32 v[186:187], v[186:187], s[36:37], s[36:37]\n"
      "v_pk_fma_f32 v[188:189], v[188:189], s[36:37], s[36:37]\n"
      "v_pk_fma_f32 v[190:191], v[190:191], s[36:37], s[36:37]\n"
      "v_rcp_f32 v184, v184\n"
      "v_rcp_f32 v185, v185\n"
      "v_rcp_f32 v186, v186\n"
      "v_rcp_f32 v187, v187\n"
      "v_rcp_f32 v188, v188\n"
      "v_rcp_f32 v189, v189\n"
      "v_rcp_f32 v190, v190\n"
      "v_rcp_f32 v191, v191\n"
      "v_pk_mul_f32 v[32:33], v[32:33], v[184:185]\n"
      "v_pk_mul_f32 v[34:35], v[34:35], v[186:187]\n"
      "v_pk_mul_f32 v[36:37], v[36:37], v[188:189]\n"
      "v_pk_mul_f32 v[38:39], v[38:39], v[190:191]\n"
      "v_cvt_pk_bf16_f32 v184, v32, v33\n"
      "v_cvt_pk_bf16_f32 v186, v36, v37\n"
      "v_cvt_pk_bf16_f32 v185, v34, v35\n"
      "v_cvt_pk_bf16_f32 v187, v38, v39\n"
      "v_permlane16_swap_b32 v184, v186\n"
      "s_nop 0\n"
      "v_permlane16_swap_b32 v185, v187\n"
      "buffer_store_dwordx4 v[184:187], v15, s[12:15], 0 offen offset:2304 sc0 sc1\n"
      "Lexit%=:\n"
      "s_waitcnt lgkmcnt(0)\n"
      "s_endpgm\n"
      "Lhelp%=:\n"
      "s_waitcnt lgkmcnt(0)\n"
      "s_add_u32 s61, s60, 0x900\n"
      "s_mul_i32 s34, s30, 0x2800\n"
      "s_add_u32 s73, s34, s35\n"
      "v_and_b32 v5, 3, v2\n"
      "v_lshrrev_b32 v6, 2, v2\n"
      "v_lshl_add_u32 v5, v5, 2, v6\n"
      "v_mul_u32_u24 v5, 0x480, v5\n"
      "v_lshl_add_u32 v11, v4, 4, v5\n"
      "s_add_u32 s62, s60, 0x12000\n"
      "s_mul_i32 s34, s28, 0x1200\n"
      "s_mul_i32 s35, s30, 0x4800\n"
      "s_add_u32 s34, s34, s35\n"
      "s_mul_i32 s35, s29, 0x900\n"
      "s_add_u32 s34, s34, s35\n"
      "s_add_u32 s64, s34, 0x24000\n"
      "s_add_u32 s63, s60, 0x12900\n"
      "v_and_b32 v6, 1, v3\n"
      "v_lshl_or_b32 v6, v6, 1, s31\n"
      "v_xor_b32 v6, v1, v6\n"
      "v_mul_u32_u24 v5, 0x480, v2\n"
      "v_lshl_add_u32 v12, v6, 4, v5\n"
      "s_lshl_b32 s33, s25, 7\n"
      "s_mul_i32 s34, s3, 0x2400\n"
      "s_add_u32 s65, s34, s33\n"
      "s_lshl_b32 s74, s3, 10\n"
      "s_mov_b32 s10, 0x1e6000\n"
      "s_mov_b32 s11, s15\n"
      "s_and_b32 s9, s9, 0xffff\n"
      "s_mul_i32 s79, s77, 0x12000\n"
      "s_add_u32 s68, s65, 36864\n"
      "s_add_u32 s66, s65, 663552\n"
      "s_add_u32 s69, s65, 700416\n"
      "s_add_u32 s67, s65, 1327104\n"
      "s_add_u32 s70, s65, 1363968\n"
      "v_and_b32 v7, 63, v0\n"
      "v_lshl_add_u32 v13, v7, 2, s33\n"
      "s_lshl_b32 s75, s3, 8\n"
      "s_movk_i32 s14, 0x480\n"
      "s_and_b32 s13, s13, 0xffff\n"
      "s_add_u32 m0, s74, 61440\n"
      "s_mov_b32 s77, 1\n"
      "s_mov_b32 s82, 8\n"
      "Lhl%=:\n"
      "s_mul_i32 s79, s77, 0x12000\n"
      "s_add_u32 s80, s65, s79\n"
      "buffer_load_dwordx4 v12, s[8:11], s80 offen lds\n"
      "s_add_u32 s80, s66, s79\n"
      "buffer_load_dwordx4 v12, s[8:11], s80 offen lds\n"
      "s_add_u32 s80, s67, s79\n"
      "buffer_load_dwordx4 v12, s[8:11], s80 offen lds\n"
      "s_add_u32 s80, s68, s79\n"
      "buffer_load_dwordx4 v12, s[8:11], s80 offen lds\n"
      "s_add_u32 s80, s69, s79\n"
      "buffer_load_dwordx4 v12, s[8:11], s80 offen lds\n"
      "s_add_u32 s80, s70, s79\n"
      "buffer_load_dwordx4 v12, s[8:11], s80 offen lds\n"
      "s_add_u32 s77, s77, 1\n"
      "s_cmp_eq_u32 s77, 9\n"
      "s_cselect_b32 s77, 0, s77\n"
      "s_sub_u32 s82, s82, 1\n"
      "s_cmp_lg_u32 s82, 0\n"
      "s_cbranch_scc1 Lhl%=\n"
      "s_waitcnt vmcnt(0)\n"
      "s_endpgm\n"
      "Lspare%=:\n"
      "s_cmp_lt_u32 s19, 30\n"
      "s_cbranch_scc1 Lexit%=\n"
      "s_cmp_ge_u32 s3, 4\n"
      "s_cbranch_scc1 Lexit%=\n"
      "s_and_b32 s20, s19, 1\n"
      "s_lshl_b32 s20, s20, 2\n"
      "s_add_u32 s20, s20, s3\n"
      "s_mul_i32 s20, s20, 0x2400\n"
      "s_sub_u32 s27, 7, s17\n"
      "s_mul_i32 s27, s27, s18\n"
      "s_bitcmp1_b32 s2, 2\n"
      "s_cselect_b32 s22, 13, 0\n"
      "s_add_u32 s27, s27, s22\n"
      "s_mul_i32 s27, s27, 0x2d000\n"
      "s_add_u32 s60, s27, s20\n"
      "s_add_u32 s22, s17, 4\n"
      "s_cmp_eq_u32 s17, s18\n"
      "s_cselect_b32 s22, s22, 0\n"
      "s_lshl_b32 s22, s22, 7\n"
      "s_add_u32 s61, s22, s20\n"
      "s_mul_i32 s62, s17, 3\n"
      "s_add_u32 s62, s62, 15\n"
      "v_and_b32 v1, 7, v0\n"
      "v_bfe_u32 v2, v0, 3, 3\n"
      "v_mul_u32_u24 v2, 0x480, v2\n"
      "v_lshl_add_u32 v10, v1, 4, v2\n"
      "s_mov_b32 s6, 0x480000\n"
      "s_mov_b32 s7, 0x20000\n"
      "s_mov_b32 s10, 0x1e6000\n"
      "s_mov_b32 s11, s7\n"
      "s_lshl_b32 m0, s3, 10\n"
      "s_waitcnt lgkmcnt(0)\n"
      "s_and_b32 s5, s5, 0xffff\n"
      "s_and_b32 s9, s9, 0xffff\n"
      "s_mov_b32 s77, 8\n"
      "s_lshl_b32 s78, s77, 7\n"
      "s_mul_i32 s79, s77, 0x12000\n"
      "s_add_u32 s80, s60, s78\n"
      "s_mov_b32 s82, s62\n"
      "Lsx0%=:\n"
      "buffer_load_dwordx4 v10, s[4:7], s80 offen lds\n"
      "s_add_u32 s80, s80, 0x12000\n"
      "s_sub_u32 s82, s82, 1\n"
      "s_cmp_lg_u32 s82, 0\n"
      "s_cbranch_scc1 Lsx0%=\n"
      "s_add_u32 s80, s61, s79\n"
      "s_sub_u32 s82, 5, s17\n"
      "Lsw0%=:\n"
      "buffer_load_dwordx4 v10, s[8:11], s80 offen lds\n"
      "s_add_u32 s83, s80, 663552\n"
      "buffer_load_dwordx4 v10, s[8:11], s83 offen lds\n"
      "s_add_u32 s83, s80, 1327104\n"
      "buffer_load_dwordx4 v10, s[8:11], s83 offen lds\n"
      "s_add_u32 s80, s80, 128\n"
      "s_sub_u32 s82, s82, 1\n"
      "s_cmp_lg_u32 s82, 0\n"
      "s_cbranch_scc1 Lsw0%=\n"
      "s_waitcnt vmcnt(16)\n"
      "s_waitcnt vmcnt(0)\n"
      "s_endpgm\n"
      :
      : "{s[0:1]}"(__builtin_amdgcn_kernarg_segment_ptr()), "{s2}"(__builtin_amdgcn_workgroup_id_x()),
        "{v0}"(__builtin_amdgcn_workitem_id_x()), "{s98}"((__attribute__((address_space(3))) char*)smem)
      : "v1", "v2", "v3", "v4", "v5", "v6", "v7", "v8", "v9", "v10", "v11", "v12", "v13", "v14", "v15", "v16", "v17", "v18", "v19", "v20", "v21", "v22", "v23", "v24", "v25", "v26", "v27", "v28", "v29", "v30", "v31", "v32", "v33", "v34", "v35", "v36", "v37", "v38", "v39", "v40", "v41", "v42", "v43", "v44", "v45", "v46", "v47", "v48", "v49", "v50", "v51", "v52", "v53", "v54", "v55", "v56", "v57", "v58", "v59", "v60", "v61", "v62", "v63", "v64", "v65", "v66", "v67", "v68", "v69", "v70", "v71", "v72", "v73", "v74", "v75", "v76", "v77", "v78", "v79", "v80", "v81", "v82", "v83", "v84", "v85", "v86", "v87", "v88", "v89", "v90", "v91", "v92", "v93", "v94", "v95", "v96", "v97", "v98", "v99", "v100", "v101", "v102", "v103", "v104", "v105", "v106", "v107", "v108", "v109", "v110", "v111", "v112", "v113", "v114", "v115", "v116", "v117", "v118", "v119", "v120", "v121", "v122", "v123", "v124", "v125", "v126", "v127", "v128", "v129", "v130", "v131", "v132", "v133", "v134", "v135", "v136", "v137", "v138", "v139", "v140", "v141", "v142", "v143", "v144", "v145", "v146", "v147", "v148", "v149", "v150", "v151", "v152", "v153", "v154", "v155", "v156", "v157", "v158", "v159", "v160", "v161", "v162", "v163", "v164", "v165", "v166", "v167", "v168", "v169", "v170", "v171", "v172", "v173", "v174", "v175", "v176", "v177", "v178", "v179", "v180", "v181", "v182", "v183", "v184", "v185", "v186", "v187", "v188", "v189", "v190", "v191", "v192", "v193", "v194", "v195", "v196", "v197", "v198", "v199", "v200", "v201", "v202", "v203", "v204", "v205", "v206", "v207", "v208", "v209", "v210", "v211", "v212", "v213", "v214", "v215", "v216", "v217", "v218", "v219", "v220", "v221", "v222", "v223", "v224", "v225", "v226", "v227", "v228", "v229", "v230", "v231", "v232", "v233", "v234", "v235", "v236", "v237", "v238", "v239", "v240", "v241", "v242", "v243", "s3", "s4", "s5", "s6", "s7", "s8", "s9", "s10", "s11", "s12", "s13", "s14", "s15", "s16", "s17", "s18", "s19", "s20", "s21", "s22", "s23", "s24", "s25", "s26", "s27", "s28", "s29", "s30", "s31", "s32", "s33", "s34", "s35", "s36", "s37", "s38", "s39", "s40", "s41", "s42", "s43", "s44", "s45", "s46", "s47", "s48", "s49", "s50", "s51", "s52", "s53", "s54", "s55", "s56", "s57", "s58", "s59", "s60", "s61", "s62", "s63", "s64", "s65", "s66", "s67", "s68", "s69", "s70", "s71", "s72", "s73", "s74", "s75", "s76", "s77", "s78", "s79", "s80", "s81", "s82", "s83", "s84", "s85", "s86", "s87", "s88", "s89", "s90", "s91", "s92", "s93", "s94", "s95", "s96", "s97", "vcc", "scc", "memory");
#endif
}

extern "C" void kernel_launch(void* const* d_in, const int* in_sizes, int n_in, void* d_out, int out_size, void* d_ws,
                              size_t ws_size, hipStream_t stream) {
  conv_bn_silu<<<256, 512, 0, stream>>>(d_in[0], d_in[1], d_in[2], d_in[3], d_in[4], d_in[5], d_out);
}
```
